```python
import torch

def _softmax(x, axis=-1):
    return torch.softmax(x, dim=axis)
def _gelu(x):
    return torch.nn.functional.gelu(x, approximate="tanh")
def _silu(x):
    return torch.nn.functional.silu(x)
def _sigmoid(x):
    return torch.sigmoid(x)
def _layernorm(x, axis=-1, eps=1e-5):
    m = x.mean(dim=axis, keepdim=True)
    v = x.var(dim=axis, keepdim=True, unbiased=False)
    return (x - m) / torch.sqrt(v + eps)
def _scatter_add(tgt, idx, src, axis=0):
    return tgt.clone().index_add_(axis, idx, src)
def _segment_sum(src, seg, n):
    out = torch.zeros((n,) + src.shape[1:], dtype=src.dtype)
    return out.index_add_(0, seg, src)
def _pad(x, pw):
    flat = tuple(v for pair in reversed(pw) for v in pair)
    return torch.nn.functional.pad(x, flat)

kernel_name = 'standalone__gqa_softmax__near_ridge'

def setup_inputs():

    _w = (lambda a: (lambda t: t.to(torch.bfloat16) if t.is_floating_point() else t)(torch.as_tensor(a)))
    B, Hq, Hkv, Sq, Sk, D = 16, 32, 4, 48, 1024, 128
    return {
        "q": _w((lambda *s: torch.randn(*s, dtype=torch.bfloat16) * 0.8)(B, Hq, Sq, D)),
        "k": _w((lambda *s: torch.randn(*s, dtype=torch.bfloat16) * 0.8)(B, Hkv, Sk, D)),
        "v": _w((lambda *s: torch.randn(*s, dtype=torch.bfloat16) * 0.05)(B, Hkv, Sk, D)),
    }

def reference(q, k, v):
    B, Hq, Sq, D = q.shape
    Hkv = k.shape[1]
    rep = Hq // Hkv
    k = (lambda x, n, axis: torch.repeat_interleave(x, n, dim=axis))(k, rep, axis=1)
    v = (lambda x, n, axis: torch.repeat_interleave(x, n, dim=axis))(v, rep, axis=1)
    scale = D ** -0.5
    scores = torch.einsum("bhqd,bhkd->bhqk", q, k) * scale
    p = _softmax(scores, axis=-1)
    return torch.einsum("bhqk,bhkd->bhqd", p, v)
```

```cpp
#include <hip/hip_runtime.h>
#include <hip/hip_bf16.h>
typedef __bf16 bf16x8 __attribute__((ext_vector_type(8)));
typedef float f32x16 __attribute__((ext_vector_type(16)));
typedef unsigned short u16;
constexpr int B=16,HQ=32,HKV=4,SQ=48,SK=1024,D=128,G=8;
__global__ __launch_bounds__(64) void attn_simple(const u16* __restrict__ Q, const u16* __restrict__ K, const u16* __restrict__ V, u16* __restrict__ O){
  int blk=blockIdx.x;
  int grp=blk/12, rt=blk%12;
  int lane=threadIdx.x, r=lane&31, h=lane>>5;
  const u16* Qg=Q+(size_t)grp*384*D + (size_t)(rt*32+r)*D;
  const u16* Kg=K+(size_t)grp*SK*D;
  const u16* Vg=V+(size_t)grp*SK*D;
  bf16x8 q[8];
  for(int s=0;s<8;s++) q[s]=*(const bf16x8*)(Qg+16*s+8*h);
  f32x16 o[4]={};
  float m=-INFINITY,l=0.f;
  const float c=0.08838834764831845f*1.4426950408889634f;
  for(int kt=0;kt<SK/32;kt++){
    f32x16 s={};
    for(int st=0;st<8;st++){
      bf16x8 kf=*(const bf16x8*)(Kg+(size_t)(kt*32+r)*D+16*st+8*h);
      s=__builtin_amdgcn_mfma_f32_32x32x16_bf16(kf,q[st],s,0,0,0);
    }
    float mx=s[0];
    for(int i=1;i<16;i++) mx=fmaxf(mx,s[i]);
    mx=fmaxf(mx,__shfl_xor(mx,32));
    mx*=c;
    float mn=fmaxf(m,mx);
    float al=exp2f(m-mn);
    m=mn;
    float ps=0;
    for(int i=0;i<16;i++){ s[i]=exp2f(s[i]*c-mn); ps+=s[i]; }
    ps+=__shfl_xor(ps,32);
    l=l*al+ps;
    for(int d=0;d<4;d++) for(int i=0;i<16;i++) o[d][i]*=al;
    for(int ks=0;ks<2;ks++){
      bf16x8 p;
      for(int j=0;j<8;j++) p[j]=(__bf16)s[8*ks+j];
      for(int d=0;d<4;d++){
        bf16x8 vf;
        for(int j=0;j<8;j++){
          int key=kt*32+16*ks+8*(j>>2)+4*h+(j&3);
          u16 x=Vg[(size_t)key*D+32*d+r];
          vf[j]=__builtin_bit_cast(__bf16,x);
        }
        o[d]=__builtin_amdgcn_mfma_f32_32x32x16_bf16(vf,p,o[d],0,0,0);
      }
    }
  }
  float il=1.f/l;
  u16* Og=O+(size_t)grp*384*D+(size_t)(rt*32+r)*D;
  for(int d=0;d<4;d++) for(int i=0;i<16;i++){
    int dd=32*d+(i&3)+8*(i>>2)+4*h;
    __bf16 x=(__bf16)(o[d][i]*il);
    Og[dd]=__builtin_bit_cast(u16,x);
  }
}
extern "C" void kernel_launch(void* const* d_in, const int* in_sizes, int n_in, void* d_out, int out_size, void* d_ws, size_t ws_size, hipStream_t stream) {
  attn_simple<<<64*12,64,0,stream>>>((const u16*)d_in[0],(const u16*)d_in[1],(const u16*)d_in[2],(u16*)d_out);
}
```

```cpp
#include <hip/hip_runtime.h>
#include <hip/hip_bf16.h>
typedef __bf16 bf16x8 __attribute__((ext_vector_type(8)));
typedef float f32x16 __attribute__((ext_vector_type(16)));
typedef unsigned short u16;
constexpr int B=16,HQ=32,HKV=4,SQ=48,SK=1024,D=128,G=8;
struct Pad { unsigned x[64]; };
__global__ __launch_bounds__(64) void attn_simple(const u16* __restrict__ Q, const u16* __restrict__ K, const u16* __restrict__ V, u16* __restrict__ O, Pad pad){
  {
    unsigned long long t0,t1; unsigned val;
    const int MODE=4;
    if (blockIdx.x==0) {
      const u16* p = MODE==1? Q+3000000 : (MODE==2? V+5000000 : K+5000000);
      if (MODE<=2) {
        asm volatile("s_memtime %0\n s_waitcnt lgkmcnt(0)\n global_load_dword %2, %3, off\n s_waitcnt vmcnt(0)\n s_memtime %1\n s_waitcnt lgkmcnt(0)" : "=&s"(t0),"=&s"(t1),"=&v"(val) : "v"(p) : "memory");
      } else if (MODE==3) {
        unsigned sv; auto ka=__builtin_amdgcn_kernarg_segment_ptr();
        asm volatile("s_memtime %0\n s_waitcnt lgkmcnt(0)\n s_load_dword %2, %3, 0x100\n s_waitcnt lgkmcnt(0)\n s_memtime %1\n s_waitcnt lgkmcnt(0)" : "=&s"(t0),"=&s"(t1),"=&s"(sv) : "s"(ka) : "memory");
      } else { t0=0; t1=500; }
      unsigned long long lat=t1-t0;
      unsigned long long s0,s1;
      asm volatile("s_memtime %0\n s_waitcnt lgkmcnt(0)" : "=s"(s0));
      do { asm volatile("s_sleep 8\n s_memtime %0\n s_waitcnt lgkmcnt(0)" : "=s"(s1)); } while (s1-s0 < lat*1000ull);
    } else {
      unsigned long long s0,s1;
      asm volatile("s_memtime %0\n s_waitcnt lgkmcnt(0)" : "=s"(s0));
      do { asm volatile("s_sleep 8\n s_memtime %0\n s_waitcnt lgkmcnt(0)" : "=s"(s1)); } while (s1-s0 < 20000ull);
    }
  }

  int blk=blockIdx.x;
  int grp=blk/12, rt=blk%12;
  int lane=threadIdx.x, r=lane&31, h=lane>>5;
  const u16* Qg=Q+(size_t)grp*384*D + (size_t)(rt*32+r)*D;
  const u16* Kg=K+(size_t)grp*SK*D;
  const u16* Vg=V+(size_t)grp*SK*D;
  bf16x8 q[8];
  for(int s=0;s<8;s++) q[s]=*(const bf16x8*)(Qg+16*s+8*h);
  f32x16 o[4]={};
  float m=-INFINITY,l=0.f;
  const float c=0.08838834764831845f*1.4426950408889634f;
  for(int kt=0;kt<SK/32;kt++){
    f32x16 s={};
    for(int st=0;st<8;st++){
      bf16x8 kf=*(const bf16x8*)(Kg+(size_t)(kt*32+r)*D+16*st+8*h);
      s=__builtin_amdgcn_mfma_f32_32x32x16_bf16(kf,q[st],s,0,0,0);
    }
    float mx=s[0];
    for(int i=1;i<16;i++) mx=fmaxf(mx,s[i]);
    mx=fmaxf(mx,__shfl_xor(mx,32));
    mx*=c;
    float mn=fmaxf(m,mx);
    float al=exp2f(m-mn);
    m=mn;
    float ps=0;
    for(int i=0;i<16;i++){ s[i]=exp2f(s[i]*c-mn); ps+=s[i]; }
    ps+=__shfl_xor(ps,32);
    l=l*al+ps;
    for(int d=0;d<4;d++) for(int i=0;i<16;i++) o[d][i]*=al;
    for(int ks=0;ks<2;ks++){
      bf16x8 p;
      for(int j=0;j<8;j++) p[j]=(__bf16)s[8*ks+j];
      for(int d=0;d<4;d++){
        bf16x8 vf;
        for(int j=0;j<8;j++){
          int key=kt*32+16*ks+8*(j>>2)+4*h+(j&3);
          u16 x=Vg[(size_t)key*D+32*d+r];
          vf[j]=__builtin_bit_cast(__bf16,x);
        }
        o[d]=__builtin_amdgcn_mfma_f32_32x32x16_bf16(vf,p,o[d],0,0,0);
      }
    }
  }
  float il=1.f/l;
  u16* Og=O+(size_t)grp*384*D+(size_t)(rt*32+r)*D;
  for(int d=0;d<4;d++) for(int i=0;i<16;i++){
    int dd=32*d+(i&3)+8*(i>>2)+4*h;
    __bf16 x=(__bf16)(o[d][i]*il);
    Og[dd]=__builtin_bit_cast(u16,x);
  }
}
extern "C" void kernel_launch(void* const* d_in, const int* in_sizes, int n_in, void* d_out, int out_size, void* d_ws, size_t ws_size, hipStream_t stream) {
  Pad pad; for(int i=0;i<64;i++) pad.x[i]=i;
  attn_simple<<<64*12,64,0,stream>>>((const u16*)d_in[0],(const u16*)d_in[1],(const u16*)d_in[2],(u16*)d_out, pad);
}
```

```cpp
#include <hip/hip_runtime.h>
#include <hip/hip_bf16.h>
typedef __bf16 bf16x8 __attribute__((ext_vector_type(8)));
typedef float f32x16 __attribute__((ext_vector_type(16)));
typedef unsigned short u16;
constexpr int B=16,HQ=32,HKV=4,SQ=48,SK=1024,D=128,G=8;
__global__ __launch_bounds__(64) void attn_simple(const u16* __restrict__ Q, const u16* __restrict__ K, const u16* __restrict__ V, u16* __restrict__ O){
  {
    unsigned long long t0,t1; unsigned val=threadIdx.x;
    if (blockIdx.x==0 || 0) {
      asm volatile("s_memtime %0\n s_waitcnt lgkmcnt(0)\n .rept 64\n v_add3_u32 %2, %2, %2, %2\n .endr\n s_memtime %1\n s_waitcnt lgkmcnt(0)" : "=&s"(t0),"=&s"(t1),"+v"(val) : : "memory");
    }
    if (blockIdx.x==0) {
      unsigned long long lat=t1-t0;
      unsigned long long s0,s1;
      asm volatile("s_memtime %0\n s_waitcnt lgkmcnt(0)" : "=s"(s0));
      do { asm volatile("s_sleep 8\n s_memtime %0\n s_waitcnt lgkmcnt(0)" : "=s"(s1)); } while (s1-s0 < lat*100ull);
    } else {
      unsigned long long s0,s1;
      asm volatile("s_memtime %0\n s_waitcnt lgkmcnt(0)" : "=s"(s0));
      do { asm volatile("s_sleep 8\n s_memtime %0\n s_waitcnt lgkmcnt(0)" : "=s"(s1)); } while (s1-s0 < 20000ull);
    }
    if (val==0x12345) O[0]=1;
  }

  int blk=blockIdx.x;
  int grp=blk/12, rt=blk%12;
  int lane=threadIdx.x, r=lane&31, h=lane>>5;
  const u16* Qg=Q+(size_t)grp*384*D + (size_t)(rt*32+r)*D;
  const u16* Kg=K+(size_t)grp*SK*D;
  const u16* Vg=V+(size_t)grp*SK*D;
  bf16x8 q[8];
  for(int s=0;s<8;s++) q[s]=*(const bf16x8*)(Qg+16*s+8*h);
  f32x16 o[4]={};
  float m=-INFINITY,l=0.f;
  const float c=0.08838834764831845f*1.4426950408889634f;
  for(int kt=0;kt<SK/32;kt++){
    f32x16 s={};
    for(int st=0;st<8;st++){
      bf16x8 kf=*(const bf16x8*)(Kg+(size_t)(kt*32+r)*D+16*st+8*h);
      s=__builtin_amdgcn_mfma_f32_32x32x16_bf16(kf,q[st],s,0,0,0);
    }
    float mx=s[0];
    for(int i=1;i<16;i++) mx=fmaxf(mx,s[i]);
    mx=fmaxf(mx,__shfl_xor(mx,32));
    mx*=c;
    float mn=fmaxf(m,mx);
    float al=exp2f(m-mn);
    m=mn;
    float ps=0;
    for(int i=0;i<16;i++){ s[i]=exp2f(s[i]*c-mn); ps+=s[i]; }
    ps+=__shfl_xor(ps,32);
    l=l*al+ps;
    for(int d=0;d<4;d++) for(int i=0;i<16;i++) o[d][i]*=al;
    for(int ks=0;ks<2;ks++){
      bf16x8 p;
      for(int j=0;j<8;j++) p[j]=(__bf16)s[8*ks+j];
      for(int d=0;d<4;d++){
        bf16x8 vf;
        for(int j=0;j<8;j++){
          int key=kt*32+16*ks+8*(j>>2)+4*h+(j&3);
          u16 x=Vg[(size_t)key*D+32*d+r];
          vf[j]=__builtin_bit_cast(__bf16,x);
        }
        o[d]=__builtin_amdgcn_mfma_f32_32x32x16_bf16(vf,p,o[d],0,0,0);
      }
    }
  }
  float il=1.f/l;
  u16* Og=O+(size_t)grp*384*D+(size_t)(rt*32+r)*D;
  for(int d=0;d<4;d++) for(int i=0;i<16;i++){
    int dd=32*d+(i&3)+8*(i>>2)+4*h;
    __bf16 x=(__bf16)(o[d][i]*il);
    Og[dd]=__builtin_bit_cast(u16,x);
  }
}
extern "C" void kernel_launch(void* const* d_in, const int* in_sizes, int n_in, void* d_out, int out_size, void* d_ws, size_t ws_size, hipStream_t stream) {
  attn_simple<<<64*12,64,0,stream>>>((const u16*)d_in[0],(const u16*)d_in[1],(const u16*)d_in[2],(u16*)d_out);
}
```

```cpp
#include <hip/hip_runtime.h>
#include <hip/hip_bf16.h>
typedef __bf16 bf16x8 __attribute__((ext_vector_type(8)));
typedef float f32x16 __attribute__((ext_vector_type(16)));
typedef unsigned short u16;
constexpr int B=16,HQ=32,HKV=4,SQ=48,SK=1024,D=128,G=8;
__global__ __launch_bounds__(64) void attn_simple(const u16* __restrict__ Q, const u16* __restrict__ K, const u16* __restrict__ V, u16* __restrict__ O){
  unsigned long long tstart; asm volatile("s_memrealtime %0\n s_waitcnt lgkmcnt(0)" : "=s"(tstart));
  __shared__ char smem_[163840]; asm volatile("s_nop 0" :: "s"((unsigned)(size_t)(__attribute__((address_space(3))) char*)smem_));

  int blk=blockIdx.x;
  int grp=blk/12, rt=blk%12;
  int lane=threadIdx.x, r=lane&31, h=lane>>5;
  const u16* Qg=Q+(size_t)grp*384*D + (size_t)(rt*32+r)*D;
  const u16* Kg=K+(size_t)grp*SK*D;
  const u16* Vg=V+(size_t)grp*SK*D;
  bf16x8 q[8];
  for(int s=0;s<8;s++) q[s]=*(const bf16x8*)(Qg+16*s+8*h);
  f32x16 o[4]={};
  float m=-INFINITY,l=0.f;
  const float c=0.08838834764831845f*1.4426950408889634f;
  for(int kt=0;kt<SK/32;kt++){
    f32x16 s={};
    for(int st=0;st<8;st++){
      bf16x8 kf=*(const bf16x8*)(Kg+(size_t)(kt*32+r)*D+16*st+8*h);
      s=__builtin_amdgcn_mfma_f32_32x32x16_bf16(kf,q[st],s,0,0,0);
    }
    float mx=s[0];
    for(int i=1;i<16;i++) mx=fmaxf(mx,s[i]);
    mx=fmaxf(mx,__shfl_xor(mx,32));
    mx*=c;
    float mn=fmaxf(m,mx);
    float al=exp2f(m-mn);
    m=mn;
    float ps=0;
    for(int i=0;i<16;i++){ s[i]=exp2f(s[i]*c-mn); ps+=s[i]; }
    ps+=__shfl_xor(ps,32);
    l=l*al+ps;
    for(int d=0;d<4;d++) for(int i=0;i<16;i++) o[d][i]*=al;
    for(int ks=0;ks<2;ks++){
      bf16x8 p;
      for(int j=0;j<8;j++) p[j]=(__bf16)s[8*ks+j];
      for(int d=0;d<4;d++){
        bf16x8 vf;
        for(int j=0;j<8;j++){
          int key=kt*32+16*ks+8*(j>>2)+4*h+(j&3);
          u16 x=Vg[(size_t)key*D+32*d+r];
          vf[j]=__builtin_bit_cast(__bf16,x);
        }
        o[d]=__builtin_amdgcn_mfma_f32_32x32x16_bf16(vf,p,o[d],0,0,0);
      }
    }
  }
  float il=1.f/l;
  u16* Og=O+(size_t)grp*384*D+(size_t)(rt*32+r)*D;
  for(int d=0;d<4;d++) for(int i=0;i<16;i++){
    int dd=32*d+(i&3)+8*(i>>2)+4*h;
    __bf16 x=(__bf16)(o[d][i]*il);
    Og[dd]=__builtin_bit_cast(u16,x);
  }
  if (blockIdx.x==0) { unsigned long long t; do { asm volatile("s_sleep 8\n s_memrealtime %0\n s_waitcnt lgkmcnt(0)" : "=s"(t)); } while (t - tstart < 40000ull); }
}
extern "C" void kernel_launch(void* const* d_in, const int* in_sizes, int n_in, void* d_out, int out_size, void* d_ws, size_t ws_size, hipStream_t stream) {
  attn_simple<<<64*12,64,0,stream>>>((const u16*)d_in[0],(const u16*)d_in[1],(const u16*)d_in[2],(u16*)d_out);
}
```

```cpp
#include <hip/hip_runtime.h>
typedef unsigned short u16;
typedef __bf16 bf16x8 __attribute__((ext_vector_type(8)));
typedef float f32x16 __attribute__((ext_vector_type(16)));
constexpr int SK = 1024, D = 128;

__device__ __forceinline__ void slow_path(const u16* __restrict__ Q, const u16* __restrict__ K, const u16* __restrict__ V, u16* __restrict__ O,
                                          unsigned wg, unsigned wave, unsigned lane) {
  if (wave >= 3) return;
  const unsigned x = wg & 7, y = wg >> 3, grp = x * 8 + (y >> 2), hp = y & 3;
  const int r = lane & 31, h = lane >> 5;
  const size_t row = (size_t)grp * 384 + hp * 96 + wave * 32 + r;
  const u16* Qg = Q + row * D;
  const u16* Kg = K + (size_t)grp * SK * D;
  const u16* Vg = V + (size_t)grp * SK * D;
  bf16x8 q[8];
#pragma unroll
  for (int s = 0; s < 8; s++) q[s] = *(const bf16x8*)(Qg + 16 * s + 8 * h);
  f32x16 o[4] = {};
  float m = -INFINITY, l = 0.f;
  const float c = 0.08838834764831845f * 1.4426950408889634f;
  for (int kt = 0; kt < SK / 32; kt++) {
    f32x16 s = {};
#pragma unroll
    for (int st = 0; st < 8; st++) {
      bf16x8 kf = *(const bf16x8*)(Kg + (size_t)(kt * 32 + r) * D + 16 * st + 8 * h);
      s = __builtin_amdgcn_mfma_f32_32x32x16_bf16(kf, q[st], s, 0, 0, 0);
    }
    float mx = s[0];
#pragma unroll
    for (int i = 1; i < 16; i++) mx = fmaxf(mx, s[i]);
    mx = fmaxf(mx, __shfl_xor(mx, 32));
    mx *= c;
    const float mn = fmaxf(m, mx);
    const float msub = (mn == -INFINITY) ? 0.f : mn;
    const float al = exp2f(m - msub);
    m = mn;
    float ps = 0.f;
#pragma unroll
    for (int i = 0; i < 16; i++) { s[i] = exp2f(s[i] * c - msub); ps += s[i]; }
    ps += __shfl_xor(ps, 32);
    l = l * al + ps;
#pragma unroll
    for (int d = 0; d < 4; d++)
#pragma unroll
      for (int i = 0; i < 16; i++) o[d][i] *= al;
#pragma unroll
    for (int ks = 0; ks < 2; ks++) {
      bf16x8 p;
#pragma unroll
      for (int j = 0; j < 8; j++) p[j] = (__bf16)s[8 * ks + j];
#pragma unroll
      for (int d = 0; d < 4; d++) {
        bf16x8 vf;
#pragma unroll
        for (int j = 0; j < 8; j++) {
          const int key = kt * 32 + 16 * ks + 8 * (j >> 2) + 4 * h + (j & 3);
          vf[j] = __builtin_bit_cast(__bf16, Vg[(size_t)key * D + 32 * d + r]);
        }
        o[d] = __builtin_amdgcn_mfma_f32_32x32x16_bf16(vf, p, o[d], 0, 0, 0);
      }
    }
  }
  const float il = 1.f / l;
  u16* Og = O + row * D;
#pragma unroll
  for (int d = 0; d < 4; d++)
#pragma unroll
    for (int i = 0; i < 16; i++) {
      const __bf16 v = (__bf16)(o[d][i] * il);
      Og[32 * d + (i & 3) + 8 * (i >> 2) + 4 * h] = __builtin_bit_cast(u16, v);
    }
}

__global__ __launch_bounds__(256, 1) void gqa_attn_fwd(const u16* Q_, const u16* K_, const u16* V_, u16* O_) {
  __shared__ __attribute__((aligned(16))) char smem[163840];
  unsigned flag;
  const auto ka = __builtin_amdgcn_kernarg_segment_ptr();
  const unsigned wave = __builtin_amdgcn_readfirstlane(threadIdx.x >> 6);
  const unsigned wg = blockIdx.x;
  const unsigned lds = (unsigned)(size_t)(__attribute__((address_space(3))) char*)smem;
  asm volatile(
"s_load_dwordx8 s[44:51], %[ka], 0x0\n"
"v_and_b32 v196, 63, %[tid]\n"
"v_lshrrev_b32 v197, 6, %[tid]\n"
"s_getpc_b64 s[52:53]\n"
"s_nop 0\n"
"v_readfirstlane_b32 s57, v197\n"
"v_lshlrev_b32 v200, 4, v196\n"
"s_mul_i32 s63, s57, 3072\n"
"v_add_u32 v200, s63, v200\n"
"global_load_dwordx4 a[240:243], v200, s[52:53] offset:0\n"
"global_load_dwordx4 a[240:243], v200, s[52:53] offset:1024\n"
"global_load_dwordx4 a[240:243], v200, s[52:53] offset:2048\n"
"v_and_b32 v198, 15, v196\n"
"v_lshrrev_b32 v199, 4, v196\n"
"v_lshrrev_b32 v201, 1, v198\n"
"v_xor_b32 v201, v201, v199\n"
"v_lshlrev_b32 v201, 5, v201\n"
"v_and_b32 v202, 1, v198\n"
"v_lshl_add_u32 v201, v202, 4, v201\n"
"v_lshl_add_u32 v192, v199, 8, v201\n"
"v_xor_b32 v193, 0x80, v192\n"
"v_add_u32 v194, 0x1000, v192\n"
"v_add_u32 v195, 0x1000, v193\n"
"s_lshl_b32 s36, s57, 15\n"
"s_add_u32 s36, s36, %[lds]\n"
"s_lshl_b32 s35, s57, 13\n"
"s_and_b32 s58, %[wg], 7\n"
"s_lshr_b32 s59, %[wg], 3\n"
"s_lshl_b32 s58, s58, 3\n"
"s_lshr_b32 s60, s59, 2\n"
"s_add_u32 s58, s58, s60\n"
"s_and_b32 s59, s59, 3\n"
"s_lshl_b32 s60, s58, 18\n"
"s_mul_i32 s61, s58, 98304\n"
"s_mul_i32 s59, s59, 24576\n"
"s_add_u32 s61, s61, s59\n"
"s_mov_b32 s56, 262144\n"
"s_mov_b32 s22, 0x40000\n"
"s_mov_b32 s23, 0x20000\n"
"s_mov_b32 s26, 0x40000\n"
"s_mov_b32 s27, 0x20000\n"
"s_mov_b32 s30, 24576\n"
"s_mov_b32 s31, 0x20000\n"
"s_lshl_b32 s62, s57, 10\n"
"v_lshlrev_b32 v203, 4, v196\n"
"v_add_u32 v203, s62, v203\n"
"s_waitcnt lgkmcnt(0)\n"
"s_add_u32 s20, s46, s60\n"
"s_addc_u32 s21, s47, 0\n"
"s_add_u32 s28, s44, s61\n"
"s_addc_u32 s29, s45, 0\n"
"s_mov_b32 m0, s36\n"
"s_nop 0\n"
"buffer_load_dwordx4 v192, s[20:23], s35 offen offset:0 lds\n"
"buffer_load_dwordx4 v193, s[20:23], s35 offen offset:1024 lds\n"
"buffer_load_dwordx4 v192, s[20:23], s35 offen offset:2048 lds\n"
"buffer_load_dwordx4 v193, s[20:23], s35 offen offset:3072 lds\n"
"s_add_u32 m0, s36, 4096\n"
"s_nop 0\n"
"buffer_load_dwordx4 v194, s[20:23], s35 offen offset:0 lds\n"
"buffer_load_dwordx4 v195, s[20:23], s35 offen offset:1024 lds\n"
"buffer_load_dwordx4 v194, s[20:23], s35 offen offset:2048 lds\n"
"buffer_load_dwordx4 v195, s[20:23], s35 offen offset:3072 lds\n"
"buffer_load_dwordx4 v[210:213], v203, s[28:31], 0 offen offset:0\n"
"s_mov_b32 s63, 4096\n"
"buffer_load_dwordx4 v[214:217], v203, s[28:31], s63 offen offset:0\n"
"s_mov_b32 s63, 8192\n"
"buffer_load_dwordx4 v[218:221], v203, s[28:31], s63 offen offset:0\n"
"s_mov_b32 s63, 12288\n"
"buffer_load_dwordx4 v[222:225], v203, s[28:31], s63 offen offset:0\n"
"s_mov_b32 s63, 16384\n"
"buffer_load_dwordx4 v[226:229], v203, s[28:31], s63 offen offset:0\n"
"s_mov_b32 s63, 20480\n"
"buffer_load_dwordx4 v[230:233], v203, s[28:31], s63 offen offset:0\n"
"s_add_u32 s24, s48, s60\n"
"s_addc_u32 s25, s49, 0\n"
"s_add_u32 s54, s50, s61\n"
"s_addc_u32 s55, s51, 0\n"
"s_add_u32 s66, s35, 0x8000\n"
"s_add_u32 s67, s35, 0x10000\n"
"s_add_u32 s52, s20, 0x18000\n"
"s_addc_u32 s53, s21, 0\n"
"s_add_u32 s60, s24, 0x10000\n"
"s_addc_u32 s61, s25, 0\n"
"s_lshr_b32 s65, %[wg], 3\n"
"s_add_u32 s58, s65, 0\n"
"s_and_b32 s59, s58, 1\n"
"s_lshl_b32 s59, s59, 12\n"
"s_bitcmp1_b32 s58, 1\n"
"s_cselect_b32 s8, s60, s52\n"
"s_cselect_b32 s9, s61, s53\n"
"s_cselect_b32 s38, 5, 4\n"
"s_cselect_b32 s64, 2, 1\n"
"s_lshl_b32 s64, s64, 13\n"
"s_add_u32 s8, s8, s59\n"
"s_addc_u32 s9, s9, 0\n"
"s_add_u32 s64, s64, s59\n"
"s_add_u32 s16, s36, s64\n"
"s_mov_b32 s10, 0x40000\n"
"s_mov_b32 s11, 0x20000\n"
"s_add_u32 s58, s65, 1\n"
"s_and_b32 s59, s58, 1\n"
"s_lshl_b32 s59, s59, 12\n"
"s_bitcmp1_b32 s58, 1\n"
"s_cselect_b32 s12, s60, s52\n"
"s_cselect_b32 s13, s61, s53\n"
"s_cselect_b32 s39, 5, 4\n"
"s_cselect_b32 s64, 2, 1\n"
"s_lshl_b32 s64, s64, 13\n"
"s_add_u32 s12, s12, s59\n"
"s_addc_u32 s13, s13, 0\n"
"s_add_u32 s64, s64, s59\n"
"s_add_u32 s17, s36, s64\n"
"s_mov_b32 s14, 0x40000\n"
"s_mov_b32 s15, 0x20000\n"
"s_add_u32 s58, s65, 2\n"
"s_and_b32 s59, s58, 1\n"
"s_lshl_b32 s59, s59, 12\n"
"s_bitcmp1_b32 s58, 1\n"
"s_cselect_b32 s44, s60, s52\n"
"s_cselect_b32 s45, s61, s53\n"
"s_cselect_b32 s68, 5, 4\n"
"s_cselect_b32 s64, 2, 1\n"
"s_lshl_b32 s64, s64, 13\n"
"s_add_u32 s44, s44, s59\n"
"s_addc_u32 s45, s45, 0\n"
"s_add_u32 s64, s64, s59\n"
"s_add_u32 s18, s36, s64\n"
"s_mov_b32 s46, 0x40000\n"
"s_mov_b32 s47, 0x20000\n"
"s_add_u32 s58, s65, 3\n"
"s_and_b32 s59, s58, 1\n"
"s_lshl_b32 s59, s59, 12\n"
"s_bitcmp1_b32 s58, 1\n"
"s_cselect_b32 s48, s60, s52\n"
"s_cselect_b32 s49, s61, s53\n"
"s_cselect_b32 s69, 5, 4\n"
"s_cselect_b32 s64, 2, 1\n"
"s_lshl_b32 s64, s64, 13\n"
"s_add_u32 s48, s48, s59\n"
"s_addc_u32 s49, s49, 0\n"
"s_add_u32 s64, s64, s59\n"
"s_add_u32 s19, s36, s64\n"
"s_mov_b32 s50, 0x40000\n"
"s_mov_b32 s51, 0x20000\n"
"v_lshrrev_b32 v204, 1, v199\n"
"v_and_b32 v205, 7, v198\n"
"v_xor_b32 v204, v204, v205\n"
"v_lshlrev_b32 v204, 5, v204\n"
"v_and_b32 v205, 1, v199\n"
"v_lshlrev_b32 v205, 4, v205\n"
"v_lshl_add_u32 v205, v198, 8, v205\n"
"s_add_u32 s64, %[lds], 131072\n"
"v_xor_b32 v206, 0, v204\n"
"v_add_u32 v206, v206, v205\n"
"v_add_u32 v180, s36, v206\n"
"v_add_u32 v206, s64, v206\n"
"v_xor_b32 v207, 64, v204\n"
"v_add_u32 v207, v207, v205\n"
"v_add_u32 v181, s36, v207\n"
"v_add_u32 v207, s64, v207\n"
"v_xor_b32 v208, 128, v204\n"
"v_add_u32 v208, v208, v205\n"
"v_add_u32 v182, s36, v208\n"
"v_add_u32 v208, s64, v208\n"
"v_xor_b32 v209, 192, v204\n"
"v_add_u32 v209, v209, v205\n"
"v_add_u32 v183, s36, v209\n"
"v_add_u32 v209, s64, v209\n"
"s_and_b32 s58, s57, 1\n"
"s_lshl_b32 s58, s58, 7\n"
"s_add_u32 s59, s64, s62\n"
"v_xor_b32 v240, s58, v192\n"
"v_add_u32 v240, s59, v240\n"
"s_mov_b32 s40, 0x3e0293ee\n"
"s_mov_b32 s41, 0x3e0293ee\n"
"s_mov_b32 s42, 0xffff0000\n"
"v_lshrrev_b32 v236, 2, v198\n"
"v_and_b32 v237, 3, v196\n"
"v_and_b32 v238, 1, v199\n"
"v_lshl_add_u32 v238, v238, 2, v236\n"
"v_lshl_add_u32 v239, v199, 2, v236\n"
"v_lshlrev_b32 v239, 8, v239\n"
"v_lshl_add_u32 v239, v237, 3, v239\n"
"s_add_u32 s58, s36, 16384\n"
"v_add_u32 v239, s58, v239\n"
"s_lshl_b32 s59, s57, 1\n"
"s_add_u32 s60, s59, 0\n"
"s_and_b32 s60, s60, 7\n"
"v_xor_b32 v184, s60, v238\n"
"v_lshl_add_u32 v184, v184, 5, v239\n"
"s_add_u32 s60, s59, 1\n"
"s_and_b32 s60, s60, 7\n"
"v_xor_b32 v185, s60, v238\n"
"v_lshl_add_u32 v185, v185, 5, v239\n"
"s_add_u32 s60, s59, 2\n"
"s_and_b32 s60, s60, 7\n"
"v_xor_b32 v186, s60, v238\n"
"v_lshl_add_u32 v186, v186, 5, v239\n"
"s_add_u32 s60, s59, 3\n"
"s_and_b32 s60, s60, 7\n"
"v_xor_b32 v187, s60, v238\n"
"v_lshl_add_u32 v187, v187, 5, v239\n"
"s_add_u32 s60, s59, 4\n"
"s_and_b32 s60, s60, 7\n"
"v_xor_b32 v188, s60, v238\n"
"v_lshl_add_u32 v188, v188, 5, v239\n"
"s_add_u32 s60, s59, 5\n"
"s_and_b32 s60, s60, 7\n"
"v_xor_b32 v189, s60, v238\n"
"v_lshl_add_u32 v189, v189, 5, v239\n"
"s_add_u32 s60, s59, 6\n"
"s_and_b32 s60, s60, 7\n"
"v_xor_b32 v190, s60, v238\n"
"v_lshl_add_u32 v190, v190, 5, v239\n"
"s_add_u32 s60, s59, 7\n"
"s_and_b32 s60, s60, 7\n"
"v_xor_b32 v191, s60, v238\n"
"v_lshl_add_u32 v191, v191, 5, v239\n"
"v_mov_b32 v176, 0\n"
"v_mov_b32 v177, 0\n"
"v_mov_b32 v178, 0\n"
"v_mov_b32 v179, 0\n"
"s_nop 1\n"
"v_mfma_f32_32x32x16_bf16 v[160:175], v[176:179], v[176:179], 0\n"
"v_mfma_f32_32x32x16_bf16 v[0:15], v[176:179], v[176:179], 0\n"
"v_mfma_f32_32x32x16_bf16 v[16:31], v[176:179], v[176:179], 0\n"
"v_mfma_f32_32x32x16_bf16 v[32:47], v[176:179], v[176:179], 0\n"
"v_mfma_f32_32x32x16_bf16 a[0:15], v[176:179], v[176:179], 0\n"
"v_mfma_f32_32x32x16_bf16 a[16:31], v[176:179], v[176:179], 0\n"
"v_mfma_f32_32x32x16_bf16 a[32:47], v[176:179], v[176:179], 0\n"
"v_mfma_f32_32x32x16_bf16 a[48:63], v[176:179], v[176:179], 0\n"
"v_mfma_f32_32x32x16_bf16 a[64:79], v[176:179], v[176:179], 0\n"
"v_mfma_f32_32x32x16_bf16 a[80:95], v[176:179], v[176:179], 0\n"
"v_mfma_f32_32x32x16_bf16 a[96:111], v[176:179], v[176:179], 0\n"
"v_mfma_f32_32x32x16_bf16 a[112:127], v[176:179], v[176:179], 0\n"
"v_mfma_f32_32x32x16_bf16 a[128:143], v[176:179], v[176:179], 0\n"
"v_mov_b32 v176, 0x3f803f80\n"
"v_mov_b32 v177, 0x3f803f80\n"
"v_mov_b32 v178, 0x3f803f80\n"
"v_mov_b32 v179, 0x3f803f80\n"
"s_waitcnt vmcnt(5)\n"
"ds_read_b128 v[48:51], v180 offset:0\n"
"ds_read_b128 v[52:55], v181 offset:0\n"
"ds_read_b128 v[56:59], v182 offset:0\n"
"ds_read_b128 v[60:63], v183 offset:0\n"
"ds_read_b128 v[64:67], v180 offset:4096\n"
"ds_read_b128 v[68:71], v181 offset:4096\n"
"ds_read_b128 v[72:75], v182 offset:4096\n"
"ds_read_b128 v[76:79], v183 offset:4096\n"
"v_lshlrev_b32 v242, 16, v210\n"
"v_and_b32 v243, s42, v210\n"
"v_pk_mul_f32 v[242:243], v[242:243], s[40:41]\n"
"v_cvt_pk_bf16_f32 v210, v242, v243\n"
"v_lshlrev_b32 v244, 16, v211\n"
"v_and_b32 v245, s42, v211\n"
"v_pk_mul_f32 v[244:245], v[244:245], s[40:41]\n"
"v_cvt_pk_bf16_f32 v211, v244, v245\n"
"v_lshlrev_b32 v246, 16, v212\n"
"v_and_b32 v247, s42, v212\n"
"v_pk_mul_f32 v[246:247], v[246:247], s[40:41]\n"
"v_cvt_pk_bf16_f32 v212, v246, v247\n"
"v_lshlrev_b32 v242, 16, v213\n"
"v_and_b32 v243, s42, v213\n"
"v_pk_mul_f32 v[242:243], v[242:243], s[40:41]\n"
"v_cvt_pk_bf16_f32 v213, v242, v243\n"
"ds_write_b128 v240, v[210:213] offset:0\n"
"s_add_u32 m0, s36, 8192\n"
"s_nop 0\n"
"buffer_load_dwordx4 v192, s[20:23], s66 offen offset:0 lds\n"
"buffer_load_dwordx4 v193, s[20:23], s66 offen offset:1024 lds\n"
"buffer_load_dwordx4 v192, s[20:23], s66 offen offset:2048 lds\n"
"s_waitcnt lgkmcnt(0)\n"
"s_barrier\n"
"ds_read_b128 a[144:147], v206 offset:0\n"
"ds_read_b128 a[148:151], v207 offset:0\n"
"ds_read_b128 a[152:155], v208 offset:0\n"
"ds_read_b128 a[156:159], v209 offset:0\n"
"s_waitcnt vmcnt(7)\n"
"v_lshlrev_b32 v242, 16, v214\n"
"v_and_b32 v243, s42, v214\n"
"v_pk_mul_f32 v[242:243], v[242:243], s[40:41]\n"
"v_cvt_pk_bf16_f32 v214, v242, v243\n"
"v_lshlrev_b32 v244, 16, v215\n"
"v_and_b32 v245, s42, v215\n"
"v_pk_mul_f32 v[244:245], v[244:245], s[40:41]\n"
"v_cvt_pk_bf16_f32 v215, v244, v245\n"
"v_lshlrev_b32 v246, 16, v216\n"
"v_and_b32 v247, s42, v216\n"
"v_pk_mul_f32 v[246:247], v[246:247], s[40:41]\n"
"v_cvt_pk_bf16_f32 v216, v246, v247\n"
"v_lshlrev_b32 v242, 16, v217\n"
"v_and_b32 v243, s42, v217\n"
"v_pk_mul_f32 v[242:243], v[242:243], s[40:41]\n"
"v_cvt_pk_bf16_f32 v217, v242, v243\n"
"ds_write_b128 v240, v[214:217] offset:4096\n"
"s_waitcnt lgkmcnt(1)\n"
"v_mfma_f32_16x16x32_bf16 v[136:139], v[48:51], a[144:147], 0\n"
"v_mfma_f32_16x16x32_bf16 v[136:139], v[52:55], a[148:151], v[136:139]\n"
"buffer_load_dwordx4 v193, s[20:23], s66 offen offset:3072 lds\n"
"v_mfma_f32_16x16x32_bf16 v[136:139], v[56:59], a[152:155], v[136:139]\n"
"v_mfma_f32_16x16x32_bf16 v[136:139], v[60:63], a[156:159], v[136:139]\n"
"v_mfma_f32_16x16x32_bf16 v[140:143], v[64:67], a[144:147], 0\n"
"s_add_u32 m0, s36, 12288\n"
"s_nop 0\n"
"buffer_load_dwordx4 v194, s[20:23], s66 offen offset:0 lds\n"
"v_mfma_f32_16x16x32_bf16 v[140:143], v[68:71], a[148:151], v[140:143]\n"
"v_mfma_f32_16x16x32_bf16 v[140:143], v[72:75], a[152:155], v[140:143]\n"
"buffer_load_dwordx4 v195, s[20:23], s66 offen offset:1024 lds\n"
"v_mfma_f32_16x16x32_bf16 v[140:143], v[76:79], a[156:159], v[140:143]\n"
"s_waitcnt lgkmcnt(0)\n"
"s_barrier\n"
"ds_read_b128 a[160:163], v206 offset:4096\n"
"ds_read_b128 a[164:167], v207 offset:4096\n"
"ds_read_b128 a[168:171], v208 offset:4096\n"
"ds_read_b128 a[172:175], v209 offset:4096\n"
"s_waitcnt vmcnt(9)\n"
"v_lshlrev_b32 v242, 16, v218\n"
"v_and_b32 v243, s42, v218\n"
"v_pk_mul_f32 v[242:243], v[242:243], s[40:41]\n"
"v_cvt_pk_bf16_f32 v218, v242, v243\n"
"v_lshlrev_b32 v244, 16, v219\n"
"v_and_b32 v245, s42, v219\n"
"v_pk_mul_f32 v[244:245], v[244:245], s[40:41]\n"
"v_cvt_pk_bf16_f32 v219, v244, v245\n"
"v_lshlrev_b32 v246, 16, v220\n"
"v_and_b32 v247, s42, v220\n"
"v_pk_mul_f32 v[246:247], v[246:247], s[40:41]\n"
"v_cvt_pk_bf16_f32 v220, v246, v247\n"
"v_lshlrev_b32 v242, 16, v221\n"
"v_and_b32 v243, s42, v221\n"
"v_pk_mul_f32 v[242:243], v[242:243], s[40:41]\n"
"v_cvt_pk_bf16_f32 v221, v242, v243\n"
"ds_write_b128 v240, v[218:221] offset:8192\n"
"s_waitcnt lgkmcnt(1)\n"
"v_mfma_f32_16x16x32_bf16 v[144:147], v[48:51], a[160:163], 0\n"
"v_exp_f32 v136, v136\n"
"v_mfma_f32_16x16x32_bf16 v[144:147], v[52:55], a[164:167], v[144:147]\n"
"buffer_load_dwordx4 v194, s[20:23], s66 offen offset:2048 lds\n"
"v_exp_f32 v137, v137\n"
"v_mfma_f32_16x16x32_bf16 v[144:147], v[56:59], a[168:171], v[144:147]\n"
"v_exp_f32 v138, v138\n"
"v_cvt_pk_bf16_f32 v112, v136, v137\n"
"v_mfma_f32_16x16x32_bf16 v[144:147], v[60:63], a[172:175], v[144:147]\n"
"v_exp_f32 v139, v139\n"
"v_mfma_f32_16x16x32_bf16 v[148:151], v[64:67], a[160:163], 0\n"
"buffer_load_dwordx4 v195, s[20:23], s66 offen offset:3072 lds\n"
"v_exp_f32 v140, v140\n"
"v_cvt_pk_bf16_f32 v113, v138, v139\n"
"v_mfma_f32_16x16x32_bf16 v[148:151], v[68:71], a[164:167], v[148:151]\n"
"v_exp_f32 v141, v141\n"
"v_mfma_f32_16x16x32_bf16 v[148:151], v[72:75], a[168:171], v[148:151]\n"
"s_add_u32 m0, s36, 16384\n"
"s_nop 0\n"
"buffer_load_dwordx4 v192, s[24:27], s35 offen offset:0 lds\n"
"v_exp_f32 v142, v142\n"
"v_cvt_pk_bf16_f32 v114, v140, v141\n"
"v_mfma_f32_16x16x32_bf16 v[148:151], v[76:79], a[172:175], v[148:151]\n"
"v_exp_f32 v143, v143\n"
"s_nop 0\n"
"v_cvt_pk_bf16_f32 v115, v142, v143\n"
"s_waitcnt lgkmcnt(0)\n"
"s_barrier\n"
"ds_read_b128 a[176:179], v206 offset:8192\n"
"ds_read_b128 a[180:183], v207 offset:8192\n"
"ds_read_b128 a[184:187], v208 offset:8192\n"
"ds_read_b128 a[188:191], v209 offset:8192\n"
"s_waitcnt vmcnt(11)\n"
"v_lshlrev_b32 v242, 16, v222\n"
"v_and_b32 v243, s42, v222\n"
"v_pk_mul_f32 v[242:243], v[242:243], s[40:41]\n"
"v_cvt_pk_bf16_f32 v222, v242, v243\n"
"v_lshlrev_b32 v244, 16, v223\n"
"v_and_b32 v245, s42, v223\n"
"v_pk_mul_f32 v[244:245], v[244:245], s[40:41]\n"
"v_cvt_pk_bf16_f32 v223, v244, v245\n"
"v_lshlrev_b32 v246, 16, v224\n"
"v_and_b32 v247, s42, v224\n"
"v_pk_mul_f32 v[246:247], v[246:247], s[40:41]\n"
"v_cvt_pk_bf16_f32 v224, v246, v247\n"
"v_lshlrev_b32 v242, 16, v225\n"
"v_and_b32 v243, s42, v225\n"
"v_pk_mul_f32 v[242:243], v[242:243], s[40:41]\n"
"v_cvt_pk_bf16_f32 v225, v242, v243\n"
"ds_write_b128 v240, v[222:225] offset:12288\n"
"s_waitcnt lgkmcnt(1)\n"
"v_mfma_f32_16x16x32_bf16 v[152:155], v[48:51], a[176:179], 0\n"
"v_exp_f32 v144, v144\n"
"v_mfma_f32_16x16x32_bf16 v[152:155], v[52:55], a[180:183], v[152:155]\n"
"buffer_load_dwordx4 v193, s[24:27], s35 offen offset:1024 lds\n"
"v_exp_f32 v145, v145\n"
"v_add_f32 v160, v160, v144\n"
"v_mfma_f32_16x16x32_bf16 v[152:155], v[56:59], a[184:187], v[152:155]\n"
"v_exp_f32 v146, v146\n"
"v_add_f32 v160, v160, v145\n"
"v_cvt_pk_bf16_f32 v116, v144, v145\n"
"v_mfma_f32_16x16x32_bf16 v[152:155], v[60:63], a[188:191], v[152:155]\n"
"v_exp_f32 v147, v147\n"
"v_add_f32 v160, v160, v146\n"
"v_mfma_f32_16x16x32_bf16 v[156:159], v[64:67], a[176:179], 0\n"
"buffer_load_dwordx4 v192, s[24:27], s35 offen offset:2048 lds\n"
"v_exp_f32 v148, v148\n"
"v_add_f32 v160, v160, v147\n"
"v_cvt_pk_bf16_f32 v117, v146, v147\n"
"v_mfma_f32_16x16x32_bf16 v[156:159], v[68:71], a[180:183], v[156:159]\n"
"v_exp_f32 v149, v149\n"
"v_add_f32 v160, v160, v148\n"
"v_mfma_f32_16x16x32_bf16 v[156:159], v[72:75], a[184:187], v[156:159]\n"
"buffer_load_dwordx4 v193, s[24:27], s35 offen offset:3072 lds\n"
"v_exp_f32 v150, v150\n"
"v_add_f32 v160, v160, v149\n"
"v_cvt_pk_bf16_f32 v118, v148, v149\n"
"v_mfma_f32_16x16x32_bf16 v[156:159], v[76:79], a[188:191], v[156:159]\n"
"v_exp_f32 v151, v151\n"
"v_add_f32 v160, v160, v150\n"
"s_nop 0\n"
"v_add_f32 v160, v160, v151\n"
"v_cvt_pk_bf16_f32 v119, v150, v151\n"
"s_waitcnt lgkmcnt(0)\n"
"s_barrier\n"
"ds_read_b128 a[192:195], v206 offset:12288\n"
"ds_read_b128 a[196:199], v207 offset:12288\n"
"ds_read_b128 a[200:203], v208 offset:12288\n"
"ds_read_b128 a[204:207], v209 offset:12288\n"
"s_waitcnt vmcnt(13)\n"
"v_lshlrev_b32 v242, 16, v226\n"
"v_and_b32 v243, s42, v226\n"
"v_pk_mul_f32 v[242:243], v[242:243], s[40:41]\n"
"v_cvt_pk_bf16_f32 v226, v242, v243\n"
"v_lshlrev_b32 v244, 16, v227\n"
"v_and_b32 v245, s42, v227\n"
"v_pk_mul_f32 v[244:245], v[244:245], s[40:41]\n"
"v_cvt_pk_bf16_f32 v227, v244, v245\n"
"v_lshlrev_b32 v246, 16, v228\n"
"v_and_b32 v247, s42, v228\n"
"v_pk_mul_f32 v[246:247], v[246:247], s[40:41]\n"
"v_cvt_pk_bf16_f32 v228, v246, v247\n"
"v_lshlrev_b32 v242, 16, v229\n"
"v_and_b32 v243, s42, v229\n"
"v_pk_mul_f32 v[242:243], v[242:243], s[40:41]\n"
"v_cvt_pk_bf16_f32 v229, v242, v243\n"
"ds_write_b128 v240, v[226:229] offset:16384\n"
"s_waitcnt lgkmcnt(1)\n"
"v_mfma_f32_16x16x32_bf16 v[136:139], v[48:51], a[192:195], 0\n"
"v_exp_f32 v152, v152\n"
"v_mfma_f32_16x16x32_bf16 v[136:139], v[52:55], a[196:199], v[136:139]\n"
"s_add_u32 m0, s36, 20480\n"
"s_nop 0\n"
"buffer_load_dwordx4 v194, s[24:27], s35 offen offset:0 lds\n"
"v_exp_f32 v153, v153\n"
"v_add_f32 v161, v161, v152\n"
"v_mfma_f32_16x16x32_bf16 v[136:139], v[56:59], a[200:203], v[136:139]\n"
"v_exp_f32 v154, v154\n"
"v_add_f32 v161, v161, v153\n"
"v_cvt_pk_bf16_f32 v120, v152, v153\n"
"v_mfma_f32_16x16x32_bf16 v[136:139], v[60:63], a[204:207], v[136:139]\n"
"v_exp_f32 v155, v155\n"
"v_add_f32 v161, v161, v154\n"
"v_mfma_f32_16x16x32_bf16 v[140:143], v[64:67], a[192:195], 0\n"
"buffer_load_dwordx4 v195, s[24:27], s35 offen offset:1024 lds\n"
"v_exp_f32 v156, v156\n"
"v_add_f32 v161, v161, v155\n"
"v_cvt_pk_bf16_f32 v121, v154, v155\n"
"v_mfma_f32_16x16x32_bf16 v[140:143], v[68:71], a[196:199], v[140:143]\n"
"v_exp_f32 v157, v157\n"
"v_add_f32 v161, v161, v156\n"
"v_mfma_f32_16x16x32_bf16 v[140:143], v[72:75], a[200:203], v[140:143]\n"
"buffer_load_dwordx4 v194, s[24:27], s35 offen offset:2048 lds\n"
"v_exp_f32 v158, v158\n"
"v_add_f32 v161, v161, v157\n"
"v_cvt_pk_bf16_f32 v122, v156, v157\n"
"v_mfma_f32_16x16x32_bf16 v[140:143], v[76:79], a[204:207], v[140:143]\n"
"v_exp_f32 v159, v159\n"
"v_add_f32 v161, v161, v158\n"
"s_nop 0\n"
"v_add_f32 v161, v161, v159\n"
"v_cvt_pk_bf16_f32 v123, v158, v159\n"
"s_waitcnt lgkmcnt(0)\n"
"s_barrier\n"
"ds_read_b128 a[208:211], v206 offset:16384\n"
"ds_read_b128 a[212:215], v207 offset:16384\n"
"ds_read_b128 a[216:219], v208 offset:16384\n"
"ds_read_b128 a[220:223], v209 offset:16384\n"
"s_waitcnt vmcnt(15)\n"
"v_lshlrev_b32 v242, 16, v230\n"
"v_and_b32 v243, s42, v230\n"
"v_pk_mul_f32 v[242:243], v[242:243], s[40:41]\n"
"v_cvt_pk_bf16_f32 v230, v242, v243\n"
"v_lshlrev_b32 v244, 16, v231\n"
"v_and_b32 v245, s42, v231\n"
"v_pk_mul_f32 v[244:245], v[244:245], s[40:41]\n"
"v_cvt_pk_bf16_f32 v231, v244, v245\n"
"v_lshlrev_b32 v246, 16, v232\n"
"v_and_b32 v247, s42, v232\n"
"v_pk_mul_f32 v[246:247], v[246:247], s[40:41]\n"
"v_cvt_pk_bf16_f32 v232, v246, v247\n"
"v_lshlrev_b32 v242, 16, v233\n"
"v_and_b32 v243, s42, v233\n"
"v_pk_mul_f32 v[242:243], v[242:243], s[40:41]\n"
"v_cvt_pk_bf16_f32 v233, v242, v243\n"
"ds_write_b128 v240, v[230:233] offset:20480\n"
"s_waitcnt lgkmcnt(1)\n"
"v_mfma_f32_16x16x32_bf16 v[144:147], v[48:51], a[208:211], 0\n"
"v_exp_f32 v136, v136\n"
"v_mfma_f32_16x16x32_bf16 v[144:147], v[52:55], a[212:215], v[144:147]\n"
"buffer_load_dwordx4 v195, s[24:27], s35 offen offset:3072 lds\n"
"v_exp_f32 v137, v137\n"
"v_add_f32 v162, v162, v136\n"
"v_mfma_f32_16x16x32_bf16 v[144:147], v[56:59], a[216:219], v[144:147]\n"
"v_exp_f32 v138, v138\n"
"v_add_f32 v162, v162, v137\n"
"v_cvt_pk_bf16_f32 v124, v136, v137\n"
"v_mfma_f32_16x16x32_bf16 v[144:147], v[60:63], a[220:223], v[144:147]\n"
"v_exp_f32 v139, v139\n"
"v_add_f32 v162, v162, v138\n"
"v_mfma_f32_16x16x32_bf16 v[148:151], v[64:67], a[208:211], 0\n"
"s_add_u32 m0, s36, 24576\n"
"s_nop 0\n"
"buffer_load_dwordx4 v192, s[24:27], s66 offen offset:0 lds\n"
"v_exp_f32 v140, v140\n"
"v_add_f32 v162, v162, v139\n"
"v_cvt_pk_bf16_f32 v125, v138, v139\n"
"v_mfma_f32_16x16x32_bf16 v[148:151], v[68:71], a[212:215], v[148:151]\n"
"v_exp_f32 v141, v141\n"
"v_add_f32 v162, v162, v140\n"
"v_mfma_f32_16x16x32_bf16 v[148:151], v[72:75], a[216:219], v[148:151]\n"
"buffer_load_dwordx4 v193, s[24:27], s66 offen offset:1024 lds\n"
"v_exp_f32 v142, v142\n"
"v_add_f32 v162, v162, v141\n"
"v_cvt_pk_bf16_f32 v126, v140, v141\n"
"v_mfma_f32_16x16x32_bf16 v[148:151], v[76:79], a[220:223], v[148:151]\n"
"v_exp_f32 v143, v143\n"
"v_add_f32 v162, v162, v142\n"
"s_nop 0\n"
"v_add_f32 v162, v162, v143\n"
"v_cvt_pk_bf16_f32 v127, v142, v143\n"
"s_waitcnt lgkmcnt(0)\n"
"s_barrier\n"
"ds_read_b128 a[224:227], v206 offset:20480\n"
"ds_read_b128 a[228:231], v207 offset:20480\n"
"ds_read_b128 a[232:235], v208 offset:20480\n"
"ds_read_b128 a[236:239], v209 offset:20480\n"
"s_waitcnt lgkmcnt(0)\n"
"v_mfma_f32_16x16x32_bf16 v[152:155], v[48:51], a[224:227], 0\n"
"v_exp_f32 v144, v144\n"
"v_mfma_f32_16x16x32_bf16 v[152:155], v[52:55], a[228:231], v[152:155]\n"
"buffer_load_dwordx4 v192, s[24:27], s66 offen offset:2048 lds\n"
"v_exp_f32 v145, v145\n"
"v_mfma_f32_16x16x32_bf16 v[152:155], v[56:59], a[232:235], v[152:155]\n"
"v_exp_f32 v146, v146\n"
"v_cvt_pk_bf16_f32 v128, v144, v145\n"
"v_mfma_f32_16x16x32_bf16 v[152:155], v[60:63], a[236:239], v[152:155]\n"
"v_exp_f32 v147, v147\n"
"v_mfma_f32_16x16x32_bf16 v[156:159], v[64:67], a[224:227], 0\n"
"buffer_load_dwordx4 v193, s[24:27], s66 offen offset:3072 lds\n"
"v_exp_f32 v148, v148\n"
"v_cvt_pk_bf16_f32 v129, v146, v147\n"
"v_mfma_f32_16x16x32_bf16 v[156:159], v[68:71], a[228:231], v[156:159]\n"
"v_exp_f32 v149, v149\n"
"v_mfma_f32_16x16x32_bf16 v[156:159], v[72:75], a[232:235], v[156:159]\n"
"s_add_u32 m0, s36, 28672\n"
"s_nop 0\n"
"buffer_load_dwordx4 v194, s[24:27], s66 offen offset:0 lds\n"
"v_exp_f32 v150, v150\n"
"v_cvt_pk_bf16_f32 v130, v148, v149\n"
"v_mfma_f32_16x16x32_bf16 v[156:159], v[76:79], a[236:239], v[156:159]\n"
"v_exp_f32 v151, v151\n"
"s_nop 0\n"
"v_cvt_pk_bf16_f32 v131, v150, v151\n"
"s_waitcnt vmcnt(13)\n"
"ds_read_b128 v[48:51], v180 offset:8192\n"
"ds_read_b128 v[52:55], v181 offset:8192\n"
"ds_read_b128 v[56:59], v182 offset:8192\n"
"ds_read_b128 v[60:63], v183 offset:8192\n"
"ds_read_b128 v[64:67], v180 offset:12288\n"
"ds_read_b128 v[68:71], v181 offset:12288\n"
"ds_read_b128 v[72:75], v182 offset:12288\n"
"ds_read_b128 v[76:79], v183 offset:12288\n"
"v_exp_f32 v152, v152\n"
"v_exp_f32 v153, v153\n"
"v_exp_f32 v154, v154\n"
"v_exp_f32 v155, v155\n"
"v_exp_f32 v156, v156\n"
"buffer_load_dwordx4 v195, s[24:27], s66 offen offset:1024 lds\n"
"buffer_load_dwordx4 v194, s[24:27], s66 offen offset:2048 lds\n"
"buffer_load_dwordx4 v195, s[24:27], s66 offen offset:3072 lds\n"
"s_mov_b32 m0, s36\n"
"s_nop 0\n"
"buffer_load_dwordx4 v192, s[20:23], s67 offen offset:0 lds\n"
"buffer_load_dwordx4 v193, s[20:23], s67 offen offset:1024 lds\n"
"buffer_load_dwordx4 v192, s[20:23], s67 offen offset:2048 lds\n"
"buffer_load_dwordx4 v193, s[20:23], s67 offen offset:3072 lds\n"
"s_add_u32 m0, s36, 4096\n"
"s_nop 0\n"
"buffer_load_dwordx4 v194, s[20:23], s67 offen offset:0 lds\n"
"buffer_load_dwordx4 v195, s[20:23], s67 offen offset:1024 lds\n"
"buffer_load_dwordx4 v194, s[20:23], s67 offen offset:2048 lds\n"
"buffer_load_dwordx4 v195, s[20:23], s67 offen offset:3072 lds\n"
"s_waitcnt vmcnt(16)\n"
"ds_read_b64_tr_b16 v[80:81], v184 offset:0\n"
"ds_read_b64_tr_b16 v[82:83], v184 offset:4096\n"
"ds_read_b64_tr_b16 v[84:85], v185 offset:0\n"
"ds_read_b64_tr_b16 v[86:87], v185 offset:4096\n"
"ds_read_b64_tr_b16 v[88:89], v186 offset:0\n"
"ds_read_b64_tr_b16 v[90:91], v186 offset:4096\n"
"ds_read_b64_tr_b16 v[92:93], v187 offset:0\n"
"ds_read_b64_tr_b16 v[94:95], v187 offset:4096\n"
"s_mov_b32 s37, 0\n"
"s_waitcnt lgkmcnt(8)\n"
"s_barrier\n"
"LLOOP_%=:\n"
"v_mfma_f32_16x16x32_bf16 v[136:139], v[48:51], a[144:147], 0\n"
"ds_read_b64_tr_b16 v[96:97], v188 offset:0\n"
"ds_read_b64_tr_b16 v[98:99], v188 offset:4096\n"
"v_mfma_f32_16x16x32_bf16 v[136:139], v[52:55], a[148:151], v[136:139]\n"
"s_cmp_le_u32 s37, s38\n"
"v_exp_f32 v157, v157\n"
"v_mfma_f32_16x16x32_bf16 v[136:139], v[56:59], a[152:155], v[136:139]\n"
"s_cselect_b32 s10, s56, 0\n"
"ds_read_b64_tr_b16 v[100:101], v189 offset:0\n"
"ds_read_b64_tr_b16 v[102:103], v189 offset:4096\n"
"v_mfma_f32_16x16x32_bf16 v[136:139], v[60:63], a[156:159], v[136:139]\n"
"s_cmp_le_u32 s37, s39\n"
"v_exp_f32 v158, v158\n"
"v_mfma_f32_16x16x32_bf16 v[140:143], v[64:67], a[144:147], 0\n"
"s_cselect_b32 s14, s56, 0\n"
"ds_read_b64_tr_b16 v[104:105], v190 offset:0\n"
"ds_read_b64_tr_b16 v[106:107], v190 offset:4096\n"
"v_mfma_f32_16x16x32_bf16 v[140:143], v[68:71], a[148:151], v[140:143]\n"
"s_cmp_le_u32 s37, s68\n"
"v_exp_f32 v159, v159\n"
"v_mfma_f32_16x16x32_bf16 v[140:143], v[72:75], a[152:155], v[140:143]\n"
"s_cselect_b32 s46, s56, 0\n"
"ds_read_b64_tr_b16 v[108:109], v191 offset:0\n"
"ds_read_b64_tr_b16 v[110:111], v191 offset:4096\n"
"v_mfma_f32_16x16x32_bf16 v[140:143], v[76:79], a[156:159], v[140:143]\n"
"s_cmp_le_u32 s37, s69\n"
"s_waitcnt lgkmcnt(8)\n"
"v_exp_f32 v136, v136\n"
"v_mfma_f32_16x16x32_bf16 v[0:3], v[80:83], v[112:115], v[0:3]\n"
"s_cselect_b32 s50, s56, 0\n"
"v_cvt_pk_bf16_f32 v132, v152, v153\n"
"v_cvt_pk_bf16_f32 v133, v154, v155\n"
"v_mfma_f32_16x16x32_bf16 v[4:7], v[84:87], v[112:115], v[4:7]\n"
"v_exp_f32 v137, v137\n"
"v_mfma_f32_16x16x32_bf16 a[0:3], v[88:91], v[112:115], a[0:3]\n"
"v_cvt_pk_bf16_f32 v134, v156, v157\n"
"v_cvt_pk_bf16_f32 v135, v158, v159\n"
"v_mfma_f32_16x16x32_bf16 a[24:27], v[92:95], v[112:115], a[24:27]\n"
"s_waitcnt lgkmcnt(0)\n"
"s_mov_b32 m0, s16\n"
"v_exp_f32 v138, v138\n"
"v_mfma_f32_16x16x32_bf16 a[48:51], v[96:99], v[112:115], a[48:51]\n"
"buffer_load_dwordx4 v192, s[8:11], s35 offen offset:0 lds\n"
"v_mfma_f32_16x16x32_bf16 a[72:75], v[100:103], v[112:115], a[72:75]\n"
"v_exp_f32 v139, v139\n"
"v_mfma_f32_16x16x32_bf16 a[96:99], v[104:107], v[112:115], a[96:99]\n"
"buffer_load_dwordx4 v193, s[8:11], s35 offen offset:1024 lds\n"
"v_mfma_f32_16x16x32_bf16 a[120:123], v[108:111], v[112:115], a[120:123]\n"
"v_exp_f32 v140, v140\n"
"v_mfma_f32_16x16x32_bf16 v[164:167], v[176:179], v[112:115], v[164:167]\n"
"buffer_load_dwordx4 v192, s[8:11], s35 offen offset:2048 lds\n"
"v_mfma_f32_16x16x32_bf16 v[144:147], v[48:51], a[160:163], 0\n"
"v_cvt_pk_bf16_f32 v112, v136, v137\n"
"v_cvt_pk_bf16_f32 v113, v138, v139\n"
"v_mfma_f32_16x16x32_bf16 v[144:147], v[52:55], a[164:167], v[144:147]\n"
"v_exp_f32 v141, v141\n"
"v_mfma_f32_16x16x32_bf16 v[144:147], v[56:59], a[168:171], v[144:147]\n"
"buffer_load_dwordx4 v193, s[8:11], s35 offen offset:3072 lds\n"
"v_mfma_f32_16x16x32_bf16 v[144:147], v[60:63], a[172:175], v[144:147]\n"
"s_mov_b32 m0, s17\n"
"v_exp_f32 v142, v142\n"
"v_mfma_f32_16x16x32_bf16 v[148:151], v[64:67], a[160:163], 0\n"
"buffer_load_dwordx4 v192, s[12:15], s35 offen offset:0 lds\n"
"v_mfma_f32_16x16x32_bf16 v[148:151], v[68:71], a[164:167], v[148:151]\n"
"v_exp_f32 v143, v143\n"
"v_mfma_f32_16x16x32_bf16 v[148:151], v[72:75], a[168:171], v[148:151]\n"
"v_cvt_pk_bf16_f32 v114, v140, v141\n"
"v_cvt_pk_bf16_f32 v115, v142, v143\n"
"v_mfma_f32_16x16x32_bf16 v[148:151], v[76:79], a[172:175], v[148:151]\n"
"v_exp_f32 v144, v144\n"
"v_mfma_f32_16x16x32_bf16 v[8:11], v[80:83], v[116:119], v[8:11]\n"
"buffer_load_dwordx4 v193, s[12:15], s35 offen offset:1024 lds\n"
"v_mfma_f32_16x16x32_bf16 v[12:15], v[84:87], v[116:119], v[12:15]\n"
"v_exp_f32 v145, v145\n"
"v_mfma_f32_16x16x32_bf16 a[4:7], v[88:91], v[116:119], a[4:7]\n"
"v_add_f32 v160, v160, v144\n"
"v_add_f32 v160, v160, v145\n"
"v_mfma_f32_16x16x32_bf16 a[28:31], v[92:95], v[116:119], a[28:31]\n"
"v_exp_f32 v146, v146\n"
"v_mfma_f32_16x16x32_bf16 a[52:55], v[96:99], v[116:119], a[52:55]\n"
"buffer_load_dwordx4 v192, s[12:15], s35 offen offset:2048 lds\n"
"v_mfma_f32_16x16x32_bf16 a[76:79], v[100:103], v[116:119], a[76:79]\n"
"v_exp_f32 v147, v147\n"
"v_mfma_f32_16x16x32_bf16 a[100:103], v[104:107], v[116:119], a[100:103]\n"
"v_add_f32 v160, v160, v146\n"
"v_add_f32 v160, v160, v147\n"
"v_mfma_f32_16x16x32_bf16 a[124:127], v[108:111], v[116:119], a[124:127]\n"
"v_exp_f32 v148, v148\n"
"v_mfma_f32_16x16x32_bf16 v[152:155], v[48:51], a[176:179], 0\n"
"v_cvt_pk_bf16_f32 v116, v144, v145\n"
"v_cvt_pk_bf16_f32 v117, v146, v147\n"
"v_mfma_f32_16x16x32_bf16 v[152:155], v[52:55], a[180:183], v[152:155]\n"
"v_exp_f32 v149, v149\n"
"v_mfma_f32_16x16x32_bf16 v[152:155], v[56:59], a[184:187], v[152:155]\n"
"v_add_f32 v160, v160, v148\n"
"v_add_f32 v160, v160, v149\n"
"v_mfma_f32_16x16x32_bf16 v[152:155], v[60:63], a[188:191], v[152:155]\n"
"v_exp_f32 v150, v150\n"
"v_mfma_f32_16x16x32_bf16 v[156:159], v[64:67], a[176:179], 0\n"
"buffer_load_dwordx4 v193, s[12:15], s35 offen offset:3072 lds\n"
"v_mfma_f32_16x16x32_bf16 v[156:159], v[68:71], a[180:183], v[156:159]\n"
"v_exp_f32 v151, v151\n"
"v_mfma_f32_16x16x32_bf16 v[156:159], v[72:75], a[184:187], v[156:159]\n"
"v_cvt_pk_bf16_f32 v118, v148, v149\n"
"v_cvt_pk_bf16_f32 v119, v150, v151\n"
"v_mfma_f32_16x16x32_bf16 v[156:159], v[76:79], a[188:191], v[156:159]\n"
"v_exp_f32 v152, v152\n"
"v_mfma_f32_16x16x32_bf16 v[16:19], v[80:83], v[120:123], v[16:19]\n"
"v_add_f32 v160, v160, v150\n"
"v_add_f32 v160, v160, v151\n"
"v_mfma_f32_16x16x32_bf16 v[20:23], v[84:87], v[120:123], v[20:23]\n"
"v_exp_f32 v153, v153\n"
"v_mfma_f32_16x16x32_bf16 a[8:11], v[88:91], v[120:123], a[8:11]\n"
"v_add_f32 v161, v161, v152\n"
"v_add_f32 v161, v161, v153\n"
"v_mfma_f32_16x16x32_bf16 a[32:35], v[92:95], v[120:123], a[32:35]\n"
"s_mov_b32 m0, s18\n"
"v_exp_f32 v154, v154\n"
"v_mfma_f32_16x16x32_bf16 a[56:59], v[96:99], v[120:123], a[56:59]\n"
"buffer_load_dwordx4 v192, s[44:47], s35 offen offset:0 lds\n"
"v_mfma_f32_16x16x32_bf16 a[80:83], v[100:103], v[120:123], a[80:83]\n"
"v_exp_f32 v155, v155\n"
"v_mfma_f32_16x16x32_bf16 a[104:107], v[104:107], v[120:123], a[104:107]\n"
"v_add_f32 v161, v161, v154\n"
"v_add_f32 v161, v161, v155\n"
"v_mfma_f32_16x16x32_bf16 a[128:131], v[108:111], v[120:123], a[128:131]\n"
"v_exp_f32 v156, v156\n"
"v_mfma_f32_16x16x32_bf16 v[136:139], v[48:51], a[192:195], 0\n"
"v_cvt_pk_bf16_f32 v120, v152, v153\n"
"v_cvt_pk_bf16_f32 v121, v154, v155\n"
"v_mfma_f32_16x16x32_bf16 v[136:139], v[52:55], a[196:199], v[136:139]\n"
"v_exp_f32 v157, v157\n"
"v_mfma_f32_16x16x32_bf16 v[136:139], v[56:59], a[200:203], v[136:139]\n"
"v_add_f32 v161, v161, v156\n"
"v_add_f32 v161, v161, v157\n"
"v_mfma_f32_16x16x32_bf16 v[136:139], v[60:63], a[204:207], v[136:139]\n"
"v_exp_f32 v158, v158\n"
"v_mfma_f32_16x16x32_bf16 v[140:143], v[64:67], a[192:195], 0\n"
"buffer_load_dwordx4 v193, s[44:47], s35 offen offset:1024 lds\n"
"v_mfma_f32_16x16x32_bf16 v[140:143], v[68:71], a[196:199], v[140:143]\n"
"v_exp_f32 v159, v159\n"
"v_mfma_f32_16x16x32_bf16 v[140:143], v[72:75], a[200:203], v[140:143]\n"
"v_cvt_pk_bf16_f32 v122, v156, v157\n"
"v_cvt_pk_bf16_f32 v123, v158, v159\n"
"v_mfma_f32_16x16x32_bf16 v[140:143], v[76:79], a[204:207], v[140:143]\n"
"v_exp_f32 v136, v136\n"
"v_mfma_f32_16x16x32_bf16 v[24:27], v[80:83], v[124:127], v[24:27]\n"
"v_add_f32 v161, v161, v158\n"
"v_add_f32 v161, v161, v159\n"
"v_mfma_f32_16x16x32_bf16 v[28:31], v[84:87], v[124:127], v[28:31]\n"
"v_exp_f32 v137, v137\n"
"v_mfma_f32_16x16x32_bf16 a[12:15], v[88:91], v[124:127], a[12:15]\n"
"v_add_f32 v162, v162, v136\n"
"v_add_f32 v162, v162, v137\n"
"v_mfma_f32_16x16x32_bf16 a[36:39], v[92:95], v[124:127], a[36:39]\n"
"v_exp_f32 v138, v138\n"
"v_mfma_f32_16x16x32_bf16 a[60:63], v[96:99], v[124:127], a[60:63]\n"
"buffer_load_dwordx4 v192, s[44:47], s35 offen offset:2048 lds\n"
"v_mfma_f32_16x16x32_bf16 a[84:87], v[100:103], v[124:127], a[84:87]\n"
"v_exp_f32 v139, v139\n"
"v_mfma_f32_16x16x32_bf16 a[108:111], v[104:107], v[124:127], a[108:111]\n"
"v_add_f32 v162, v162, v138\n"
"v_add_f32 v162, v162, v139\n"
"v_mfma_f32_16x16x32_bf16 a[132:135], v[108:111], v[124:127], a[132:135]\n"
"v_exp_f32 v140, v140\n"
"v_mfma_f32_16x16x32_bf16 v[144:147], v[48:51], a[208:211], 0\n"
"v_cvt_pk_bf16_f32 v124, v136, v137\n"
"v_cvt_pk_bf16_f32 v125, v138, v139\n"
"v_mfma_f32_16x16x32_bf16 v[144:147], v[52:55], a[212:215], v[144:147]\n"
"v_exp_f32 v141, v141\n"
"v_mfma_f32_16x16x32_bf16 v[144:147], v[56:59], a[216:219], v[144:147]\n"
"v_add_f32 v162, v162, v140\n"
"v_add_f32 v162, v162, v141\n"
"v_mfma_f32_16x16x32_bf16 v[144:147], v[60:63], a[220:223], v[144:147]\n"
"v_exp_f32 v142, v142\n"
"v_mfma_f32_16x16x32_bf16 v[148:151], v[64:67], a[208:211], 0\n"
"buffer_load_dwordx4 v193, s[44:47], s35 offen offset:3072 lds\n"
"v_mfma_f32_16x16x32_bf16 v[148:151], v[68:71], a[212:215], v[148:151]\n"
"v_exp_f32 v143, v143\n"
"v_mfma_f32_16x16x32_bf16 v[148:151], v[72:75], a[216:219], v[148:151]\n"
"v_cvt_pk_bf16_f32 v126, v140, v141\n"
"v_cvt_pk_bf16_f32 v127, v142, v143\n"
"v_mfma_f32_16x16x32_bf16 v[148:151], v[76:79], a[220:223], v[148:151]\n"
"v_exp_f32 v144, v144\n"
"v_mfma_f32_16x16x32_bf16 v[32:35], v[80:83], v[128:131], v[32:35]\n"
"v_add_f32 v162, v162, v142\n"
"v_add_f32 v162, v162, v143\n"
"v_mfma_f32_16x16x32_bf16 v[36:39], v[84:87], v[128:131], v[36:39]\n"
"s_mov_b32 m0, s19\n"
"v_exp_f32 v145, v145\n"
"v_mfma_f32_16x16x32_bf16 a[16:19], v[88:91], v[128:131], a[16:19]\n"
"buffer_load_dwordx4 v192, s[48:51], s35 offen offset:0 lds\n"
"v_mfma_f32_16x16x32_bf16 a[40:43], v[92:95], v[128:131], a[40:43]\n"
"v_exp_f32 v146, v146\n"
"v_mfma_f32_16x16x32_bf16 a[64:67], v[96:99], v[128:131], a[64:67]\n"
"buffer_load_dwordx4 v193, s[48:51], s35 offen offset:1024 lds\n"
"v_mfma_f32_16x16x32_bf16 a[88:91], v[100:103], v[128:131], a[88:91]\n"
"v_exp_f32 v147, v147\n"
"v_mfma_f32_16x16x32_bf16 a[112:115], v[104:107], v[128:131], a[112:115]\n"
"buffer_load_dwordx4 v192, s[48:51], s35 offen offset:2048 lds\n"
"v_mfma_f32_16x16x32_bf16 a[136:139], v[108:111], v[128:131], a[136:139]\n"
"v_exp_f32 v148, v148\n"
"v_mfma_f32_16x16x32_bf16 v[168:171], v[176:179], v[128:131], v[168:171]\n"
"buffer_load_dwordx4 v193, s[48:51], s35 offen offset:3072 lds\n"
"v_mfma_f32_16x16x32_bf16 v[152:155], v[48:51], a[224:227], 0\n"
"v_cvt_pk_bf16_f32 v128, v144, v145\n"
"v_cvt_pk_bf16_f32 v129, v146, v147\n"
"v_mfma_f32_16x16x32_bf16 v[152:155], v[52:55], a[228:231], v[152:155]\n"
"s_waitcnt vmcnt(16)\n"
"v_exp_f32 v149, v149\n"
"v_mfma_f32_16x16x32_bf16 v[152:155], v[56:59], a[232:235], v[152:155]\n"
"ds_read_b128 v[48:51], v180 offset:0\n"
"ds_read_b128 v[52:55], v181 offset:0\n"
"v_mfma_f32_16x16x32_bf16 v[152:155], v[60:63], a[236:239], v[152:155]\n"
"s_xor_b32 s16, s16, 0x2000\n"
"v_exp_f32 v150, v150\n"
"v_mfma_f32_16x16x32_bf16 v[156:159], v[64:67], a[224:227], 0\n"
"ds_read_b128 v[56:59], v182 offset:0\n"
"ds_read_b128 v[60:63], v183 offset:0\n"
"v_cvt_pk_bf16_f32 v130, v148, v149\n"
"v_mfma_f32_16x16x32_bf16 v[156:159], v[68:71], a[228:231], v[156:159]\n"
"s_xor_b32 s17, s17, 0x2000\n"
"v_exp_f32 v151, v151\n"
"v_mfma_f32_16x16x32_bf16 v[156:159], v[72:75], a[232:235], v[156:159]\n"
"ds_read_b128 v[64:67], v180 offset:4096\n"
"ds_read_b128 v[68:71], v181 offset:4096\n"
"v_cvt_pk_bf16_f32 v131, v150, v151\n"
"v_mfma_f32_16x16x32_bf16 v[156:159], v[76:79], a[236:239], v[156:159]\n"
"s_xor_b32 s18, s18, 0x2000\n"
"v_exp_f32 v152, v152\n"
"v_mfma_f32_16x16x32_bf16 v[40:43], v[80:83], v[132:135], v[40:43]\n"
"ds_read_b128 v[72:75], v182 offset:4096\n"
"ds_read_b128 v[76:79], v183 offset:4096\n"
"v_mfma_f32_16x16x32_bf16 v[44:47], v[84:87], v[132:135], v[44:47]\n"
"s_xor_b32 s19, s19, 0x2000\n"
"v_exp_f32 v153, v153\n"
"v_mfma_f32_16x16x32_bf16 a[20:23], v[88:91], v[132:135], a[20:23]\n"
"ds_read_b64_tr_b16 v[80:81], v184 offset:8192\n"
"ds_read_b64_tr_b16 v[82:83], v184 offset:12288\n"
"v_mfma_f32_16x16x32_bf16 a[44:47], v[92:95], v[132:135], a[44:47]\n"
"s_add_u32 s37, s37, 1\n"
"v_exp_f32 v154, v154\n"
"v_mfma_f32_16x16x32_bf16 a[68:71], v[96:99], v[132:135], a[68:71]\n"
"ds_read_b64_tr_b16 v[84:85], v185 offset:8192\n"
"ds_read_b64_tr_b16 v[86:87], v185 offset:12288\n"
"v_mfma_f32_16x16x32_bf16 a[92:95], v[100:103], v[132:135], a[92:95]\n"
"s_add_u32 s35, s35, 0x8000\n"
"v_exp_f32 v155, v155\n"
"v_mfma_f32_16x16x32_bf16 a[116:119], v[104:107], v[132:135], a[116:119]\n"
"ds_read_b64_tr_b16 v[88:89], v186 offset:8192\n"
"ds_read_b64_tr_b16 v[90:91], v186 offset:12288\n"
"v_mfma_f32_16x16x32_bf16 a[140:143], v[108:111], v[132:135], a[140:143]\n"
"s_waitcnt lgkmcnt(6)\n"
"v_exp_f32 v156, v156\n"
"v_mfma_f32_16x16x32_bf16 v[172:175], v[176:179], v[132:135], v[172:175]\n"
"ds_read_b64_tr_b16 v[92:93], v187 offset:8192\n"
"ds_read_b64_tr_b16 v[94:95], v187 offset:12288\n"
"s_cmp_eq_u32 s37, 7\n"
"s_cbranch_scc1 LTAIL_%=\n"
"LODD_%=:\n"
"v_mfma_f32_16x16x32_bf16 v[136:139], v[48:51], a[144:147], 0\n"
"ds_read_b64_tr_b16 v[96:97], v188 offset:8192\n"
"ds_read_b64_tr_b16 v[98:99], v188 offset:12288\n"
"v_mfma_f32_16x16x32_bf16 v[136:139], v[52:55], a[148:151], v[136:139]\n"
"s_cmp_le_u32 s37, s38\n"
"v_exp_f32 v157, v157\n"
"v_mfma_f32_16x16x32_bf16 v[136:139], v[56:59], a[152:155], v[136:139]\n"
"s_cselect_b32 s10, s56, 0\n"
"ds_read_b64_tr_b16 v[100:101], v189 offset:8192\n"
"ds_read_b64_tr_b16 v[102:103], v189 offset:12288\n"
"v_mfma_f32_16x16x32_bf16 v[136:139], v[60:63], a[156:159], v[136:139]\n"
"s_cmp_le_u32 s37, s39\n"
"v_exp_f32 v158, v158\n"
"v_mfma_f32_16x16x32_bf16 v[140:143], v[64:67], a[144:147], 0\n"
"s_cselect_b32 s14, s56, 0\n"
"ds_read_b64_tr_b16 v[104:105], v190 offset:8192\n"
"ds_read_b64_tr_b16 v[106:107], v190 offset:12288\n"
"v_mfma_f32_16x16x32_bf16 v[140:143], v[68:71], a[148:151], v[140:143]\n"
"s_cmp_le_u32 s37, s68\n"
"v_exp_f32 v159, v159\n"
"v_mfma_f32_16x16x32_bf16 v[140:143], v[72:75], a[152:155], v[140:143]\n"
"s_cselect_b32 s46, s56, 0\n"
"ds_read_b64_tr_b16 v[108:109], v191 offset:8192\n"
"ds_read_b64_tr_b16 v[110:111], v191 offset:12288\n"
"v_mfma_f32_16x16x32_bf16 v[140:143], v[76:79], a[156:159], v[140:143]\n"
"s_cmp_le_u32 s37, s69\n"
"s_waitcnt lgkmcnt(8)\n"
"v_exp_f32 v136, v136\n"
"v_mfma_f32_16x16x32_bf16 v[0:3], v[80:83], v[112:115], v[0:3]\n"
"s_cselect_b32 s50, s56, 0\n"
"v_cvt_pk_bf16_f32 v132, v152, v153\n"
"v_cvt_pk_bf16_f32 v133, v154, v155\n"
"v_mfma_f32_16x16x32_bf16 v[4:7], v[84:87], v[112:115], v[4:7]\n"
"v_exp_f32 v137, v137\n"
"v_mfma_f32_16x16x32_bf16 a[0:3], v[88:91], v[112:115], a[0:3]\n"
"v_cvt_pk_bf16_f32 v134, v156, v157\n"
"v_cvt_pk_bf16_f32 v135, v158, v159\n"
"v_mfma_f32_16x16x32_bf16 a[24:27], v[92:95], v[112:115], a[24:27]\n"
"s_waitcnt lgkmcnt(0)\n"
"s_mov_b32 m0, s16\n"
"v_exp_f32 v138, v138\n"
"v_mfma_f32_16x16x32_bf16 a[48:51], v[96:99], v[112:115], a[48:51]\n"
"buffer_load_dwordx4 v192, s[8:11], s35 offen offset:0 lds\n"
"v_mfma_f32_16x16x32_bf16 a[72:75], v[100:103], v[112:115], a[72:75]\n"
"v_exp_f32 v139, v139\n"
"v_mfma_f32_16x16x32_bf16 a[96:99], v[104:107], v[112:115], a[96:99]\n"
"buffer_load_dwordx4 v193, s[8:11], s35 offen offset:1024 lds\n"
"v_mfma_f32_16x16x32_bf16 a[120:123], v[108:111], v[112:115], a[120:123]\n"
"v_exp_f32 v140, v140\n"
"v_mfma_f32_16x16x32_bf16 v[164:167], v[176:179], v[112:115], v[164:167]\n"
"buffer_load_dwordx4 v192, s[8:11], s35 offen offset:2048 lds\n"
"v_mfma_f32_16x16x32_bf16 v[144:147], v[48:51], a[160:163], 0\n"
"v_cvt_pk_bf16_f32 v112, v136, v137\n"
"v_cvt_pk_bf16_f32 v113, v138, v139\n"
"v_mfma_f32_16x16x32_bf16 v[144:147], v[52:55], a[164:167], v[144:147]\n"
"v_exp_f32 v141, v141\n"
"v_mfma_f32_16x16x32_bf16 v[144:147], v[56:59], a[168:171], v[144:147]\n"
"buffer_load_dwordx4 v193, s[8:11], s35 offen offset:3072 lds\n"
"v_mfma_f32_16x16x32_bf16 v[144:147], v[60:63], a[172:175], v[144:147]\n"
"s_mov_b32 m0, s17\n"
"v_exp_f32 v142, v142\n"
"v_mfma_f32_16x16x32_bf16 v[148:151], v[64:67], a[160:163], 0\n"
"buffer_load_dwordx4 v192, s[12:15], s35 offen offset:0 lds\n"
"v_mfma_f32_16x16x32_bf16 v[148:151], v[68:71], a[164:167], v[148:151]\n"
"v_exp_f32 v143, v143\n"
"v_mfma_f32_16x16x32_bf16 v[148:151], v[72:75], a[168:171], v[148:151]\n"
"v_cvt_pk_bf16_f32 v114, v140, v141\n"
"v_cvt_pk_bf16_f32 v115, v142, v143\n"
"v_mfma_f32_16x16x32_bf16 v[148:151], v[76:79], a[172:175], v[148:151]\n"
"v_exp_f32 v144, v144\n"
"v_mfma_f32_16x16x32_bf16 v[8:11], v[80:83], v[116:119], v[8:11]\n"
"buffer_load_dwordx4 v193, s[12:15], s35 offen offset:1024 lds\n"
"v_mfma_f32_16x16x32_bf16 v[12:15], v[84:87], v[116:119], v[12:15]\n"
"v_exp_f32 v145, v145\n"
"v_mfma_f32_16x16x32_bf16 a[4:7], v[88:91], v[116:119], a[4:7]\n"
"v_add_f32 v160, v160, v144\n"
"v_add_f32 v160, v160, v145\n"
"v_mfma_f32_16x16x32_bf16 a[28:31], v[92:95], v[116:119], a[28:31]\n"
"v_exp_f32 v146, v146\n"
"v_mfma_f32_16x16x32_bf16 a[52:55], v[96:99], v[116:119], a[52:55]\n"
"buffer_load_dwordx4 v192, s[12:15], s35 offen offset:2048 lds\n"
"v_mfma_f32_16x16x32_bf16 a[76:79], v[100:103], v[116:119], a[76:79]\n"
"v_exp_f32 v147, v147\n"
"v_mfma_f32_16x16x32_bf16 a[100:103], v[104:107], v[116:119], a[100:103]\n"
"v_add_f32 v160, v160, v146\n"
"v_add_f32 v160, v160, v147\n"
"v_mfma_f32_16x16x32_bf16 a[124:127], v[108:111], v[116:119], a[124:127]\n"
"v_exp_f32 v148, v148\n"
"v_mfma_f32_16x16x32_bf16 v[152:155], v[48:51], a[176:179], 0\n"
"v_cvt_pk_bf16_f32 v116, v144, v145\n"
"v_cvt_pk_bf16_f32 v117, v146, v147\n"
"v_mfma_f32_16x16x32_bf16 v[152:155], v[52:55], a[180:183], v[152:155]\n"
"v_exp_f32 v149, v149\n"
"v_mfma_f32_16x16x32_bf16 v[152:155], v[56:59], a[184:187], v[152:155]\n"
"v_add_f32 v160, v160, v148\n"
"v_add_f32 v160, v160, v149\n"
"v_mfma_f32_16x16x32_bf16 v[152:155], v[60:63], a[188:191], v[152:155]\n"
"v_exp_f32 v150, v150\n"
"v_mfma_f32_16x16x32_bf16 v[156:159], v[64:67], a[176:179], 0\n"
"buffer_load_dwordx4 v193, s[12:15], s35 offen offset:3072 lds\n"
"v_mfma_f32_16x16x32_bf16 v[156:159], v[68:71], a[180:183], v[156:159]\n"
"v_exp_f32 v151, v151\n"
"v_mfma_f32_16x16x32_bf16 v[156:159], v[72:75], a[184:187], v[156:159]\n"
"v_cvt_pk_bf16_f32 v118, v148, v149\n"
"v_cvt_pk_bf16_f32 v119, v150, v151\n"
"v_mfma_f32_16x16x32_bf16 v[156:159], v[76:79], a[188:191], v[156:159]\n"
"v_exp_f32 v152, v152\n"
"v_mfma_f32_16x16x32_bf16 v[16:19], v[80:83], v[120:123], v[16:19]\n"
"v_add_f32 v160, v160, v150\n"
"v_add_f32 v160, v160, v151\n"
"v_mfma_f32_16x16x32_bf16 v[20:23], v[84:87], v[120:123], v[20:23]\n"
"v_exp_f32 v153, v153\n"
"v_mfma_f32_16x16x32_bf16 a[8:11], v[88:91], v[120:123], a[8:11]\n"
"v_add_f32 v161, v161, v152\n"
"v_add_f32 v161, v161, v153\n"
"v_mfma_f32_16x16x32_bf16 a[32:35], v[92:95], v[120:123], a[32:35]\n"
"s_mov_b32 m0, s18\n"
"v_exp_f32 v154, v154\n"
"v_mfma_f32_16x16x32_bf16 a[56:59], v[96:99], v[120:123], a[56:59]\n"
"buffer_load_dwordx4 v192, s[44:47], s35 offen offset:0 lds\n"
"v_mfma_f32_16x16x32_bf16 a[80:83], v[100:103], v[120:123], a[80:83]\n"
"v_exp_f32 v155, v155\n"
"v_mfma_f32_16x16x32_bf16 a[104:107], v[104:107], v[120:123], a[104:107]\n"
"v_add_f32 v161, v161, v154\n"
"v_add_f32 v161, v161, v155\n"
"v_mfma_f32_16x16x32_bf16 a[128:131], v[108:111], v[120:123], a[128:131]\n"
"v_exp_f32 v156, v156\n"
"v_mfma_f32_16x16x32_bf16 v[136:139], v[48:51], a[192:195], 0\n"
"v_cvt_pk_bf16_f32 v120, v152, v153\n"
"v_cvt_pk_bf16_f32 v121, v154, v155\n"
"v_mfma_f32_16x16x32_bf16 v[136:139], v[52:55], a[196:199], v[136:139]\n"
"v_exp_f32 v157, v157\n"
"v_mfma_f32_16x16x32_bf16 v[136:139], v[56:59], a[200:203], v[136:139]\n"
"v_add_f32 v161, v161, v156\n"
"v_add_f32 v161, v161, v157\n"
"v_mfma_f32_16x16x32_bf16 v[136:139], v[60:63], a[204:207], v[136:139]\n"
"v_exp_f32 v158, v158\n"
"v_mfma_f32_16x16x32_bf16 v[140:143], v[64:67], a[192:195], 0\n"
"buffer_load_dwordx4 v193, s[44:47], s35 offen offset:1024 lds\n"
"v_mfma_f32_16x16x32_bf16 v[140:143], v[68:71], a[196:199], v[140:143]\n"
"v_exp_f32 v159, v159\n"
"v_mfma_f32_16x16x32_bf16 v[140:143], v[72:75], a[200:203], v[140:143]\n"
"v_cvt_pk_bf16_f32 v122, v156, v157\n"
"v_cvt_pk_bf16_f32 v123, v158, v159\n"
"v_mfma_f32_16x16x32_bf16 v[140:143], v[76:79], a[204:207], v[140:143]\n"
"v_exp_f32 v136, v136\n"
"v_mfma_f32_16x16x32_bf16 v[24:27], v[80:83], v[124:127], v[24:27]\n"
"v_add_f32 v161, v161, v158\n"
"v_add_f32 v161, v161, v159\n"
"v_mfma_f32_16x16x32_bf16 v[28:31], v[84:87], v[124:127], v[28:31]\n"
"v_exp_f32 v137, v137\n"
"v_mfma_f32_16x16x32_bf16 a[12:15], v[88:91], v[124:127], a[12:15]\n"
"v_add_f32 v162, v162, v136\n"
"v_add_f32 v162, v162, v137\n"
"v_mfma_f32_16x16x32_bf16 a[36:39], v[92:95], v[124:127], a[36:39]\n"
"v_exp_f32 v138, v138\n"
"v_mfma_f32_16x16x32_bf16 a[60:63], v[96:99], v[124:127], a[60:63]\n"
"buffer_load_dwordx4 v192, s[44:47], s35 offen offset:2048 lds\n"
"v_mfma_f32_16x16x32_bf16 a[84:87], v[100:103], v[124:127], a[84:87]\n"
"v_exp_f32 v139, v139\n"
"v_mfma_f32_16x16x32_bf16 a[108:111], v[104:107], v[124:127], a[108:111]\n"
"v_add_f32 v162, v162, v138\n"
"v_add_f32 v162, v162, v139\n"
"v_mfma_f32_16x16x32_bf16 a[132:135], v[108:111], v[124:127], a[132:135]\n"
"v_exp_f32 v140, v140\n"
"v_mfma_f32_16x16x32_bf16 v[144:147], v[48:51], a[208:211], 0\n"
"v_cvt_pk_bf16_f32 v124, v136, v137\n"
"v_cvt_pk_bf16_f32 v125, v138, v139\n"
"v_mfma_f32_16x16x32_bf16 v[144:147], v[52:55], a[212:215], v[144:147]\n"
"v_exp_f32 v141, v141\n"
"v_mfma_f32_16x16x32_bf16 v[144:147], v[56:59], a[216:219], v[144:147]\n"
"v_add_f32 v162, v162, v140\n"
"v_add_f32 v162, v162, v141\n"
"v_mfma_f32_16x16x32_bf16 v[144:147], v[60:63], a[220:223], v[144:147]\n"
"v_exp_f32 v142, v142\n"
"v_mfma_f32_16x16x32_bf16 v[148:151], v[64:67], a[208:211], 0\n"
"buffer_load_dwordx4 v193, s[44:47], s35 offen offset:3072 lds\n"
"v_mfma_f32_16x16x32_bf16 v[148:151], v[68:71], a[212:215], v[148:151]\n"
"v_exp_f32 v143, v143\n"
"v_mfma_f32_16x16x32_bf16 v[148:151], v[72:75], a[216:219], v[148:151]\n"
"v_cvt_pk_bf16_f32 v126, v140, v141\n"
"v_cvt_pk_bf16_f32 v127, v142, v143\n"
"v_mfma_f32_16x16x32_bf16 v[148:151], v[76:79], a[220:223], v[148:151]\n"
"v_exp_f32 v144, v144\n"
"v_mfma_f32_16x16x32_bf16 v[32:35], v[80:83], v[128:131], v[32:35]\n"
"v_add_f32 v162, v162, v142\n"
"v_add_f32 v162, v162, v143\n"
"v_mfma_f32_16x16x32_bf16 v[36:39], v[84:87], v[128:131], v[36:39]\n"
"s_mov_b32 m0, s19\n"
"v_exp_f32 v145, v145\n"
"v_mfma_f32_16x16x32_bf16 a[16:19], v[88:91], v[128:131], a[16:19]\n"
"buffer_load_dwordx4 v192, s[48:51], s35 offen offset:0 lds\n"
"v_mfma_f32_16x16x32_bf16 a[40:43], v[92:95], v[128:131], a[40:43]\n"
"v_exp_f32 v146, v146\n"
"v_mfma_f32_16x16x32_bf16 a[64:67], v[96:99], v[128:131], a[64:67]\n"
"buffer_load_dwordx4 v193, s[48:51], s35 offen offset:1024 lds\n"
"v_mfma_f32_16x16x32_bf16 a[88:91], v[100:103], v[128:131], a[88:91]\n"
"v_exp_f32 v147, v147\n"
"v_mfma_f32_16x16x32_bf16 a[112:115], v[104:107], v[128:131], a[112:115]\n"
"buffer_load_dwordx4 v192, s[48:51], s35 offen offset:2048 lds\n"
"v_mfma_f32_16x16x32_bf16 a[136:139], v[108:111], v[128:131], a[136:139]\n"
"v_exp_f32 v148, v148\n"
"v_mfma_f32_16x16x32_bf16 v[168:171], v[176:179], v[128:131], v[168:171]\n"
"buffer_load_dwordx4 v193, s[48:51], s35 offen offset:3072 lds\n"
"v_mfma_f32_16x16x32_bf16 v[152:155], v[48:51], a[224:227], 0\n"
"v_cvt_pk_bf16_f32 v128, v144, v145\n"
"v_cvt_pk_bf16_f32 v129, v146, v147\n"
"v_mfma_f32_16x16x32_bf16 v[152:155], v[52:55], a[228:231], v[152:155]\n"
"s_waitcnt vmcnt(16)\n"
"v_exp_f32 v149, v149\n"
"v_mfma_f32_16x16x32_bf16 v[152:155], v[56:59], a[232:235], v[152:155]\n"
"ds_read_b128 v[48:51], v180 offset:8192\n"
"ds_read_b128 v[52:55], v181 offset:8192\n"
"v_mfma_f32_16x16x32_bf16 v[152:155], v[60:63], a[236:239], v[152:155]\n"
"s_xor_b32 s16, s16, 0x2000\n"
"v_exp_f32 v150, v150\n"
"v_mfma_f32_16x16x32_bf16 v[156:159], v[64:67], a[224:227], 0\n"
"ds_read_b128 v[56:59], v182 offset:8192\n"
"ds_read_b128 v[60:63], v183 offset:8192\n"
"v_cvt_pk_bf16_f32 v130, v148, v149\n"
"v_mfma_f32_16x16x32_bf16 v[156:159], v[68:71], a[228:231], v[156:159]\n"
"s_xor_b32 s17, s17, 0x2000\n"
"v_exp_f32 v151, v151\n"
"v_mfma_f32_16x16x32_bf16 v[156:159], v[72:75], a[232:235], v[156:159]\n"
"ds_read_b128 v[64:67], v180 offset:12288\n"
"ds_read_b128 v[68:71], v181 offset:12288\n"
"v_cvt_pk_bf16_f32 v131, v150, v151\n"
"v_mfma_f32_16x16x32_bf16 v[156:159], v[76:79], a[236:239], v[156:159]\n"
"s_xor_b32 s18, s18, 0x2000\n"
"v_exp_f32 v152, v152\n"
"v_mfma_f32_16x16x32_bf16 v[40:43], v[80:83], v[132:135], v[40:43]\n"
"ds_read_b128 v[72:75], v182 offset:12288\n"
"ds_read_b128 v[76:79], v183 offset:12288\n"
"v_mfma_f32_16x16x32_bf16 v[44:47], v[84:87], v[132:135], v[44:47]\n"
"s_xor_b32 s19, s19, 0x2000\n"
"v_exp_f32 v153, v153\n"
"v_mfma_f32_16x16x32_bf16 a[20:23], v[88:91], v[132:135], a[20:23]\n"
"ds_read_b64_tr_b16 v[80:81], v184 offset:0\n"
"ds_read_b64_tr_b16 v[82:83], v184 offset:4096\n"
"v_mfma_f32_16x16x32_bf16 a[44:47], v[92:95], v[132:135], a[44:47]\n"
"s_add_u32 s37, s37, 1\n"
"v_exp_f32 v154, v154\n"
"v_mfma_f32_16x16x32_bf16 a[68:71], v[96:99], v[132:135], a[68:71]\n"
"ds_read_b64_tr_b16 v[84:85], v185 offset:0\n"
"ds_read_b64_tr_b16 v[86:87], v185 offset:4096\n"
"v_mfma_f32_16x16x32_bf16 a[92:95], v[100:103], v[132:135], a[92:95]\n"
"s_add_u32 s35, s35, 0x8000\n"
"v_exp_f32 v155, v155\n"
"v_mfma_f32_16x16x32_bf16 a[116:119], v[104:107], v[132:135], a[116:119]\n"
"ds_read_b64_tr_b16 v[88:89], v186 offset:0\n"
"ds_read_b64_tr_b16 v[90:91], v186 offset:4096\n"
"v_mfma_f32_16x16x32_bf16 a[140:143], v[108:111], v[132:135], a[140:143]\n"
"s_waitcnt lgkmcnt(6)\n"
"v_exp_f32 v156, v156\n"
"v_mfma_f32_16x16x32_bf16 v[172:175], v[176:179], v[132:135], v[172:175]\n"
"ds_read_b64_tr_b16 v[92:93], v187 offset:0\n"
"ds_read_b64_tr_b16 v[94:95], v187 offset:4096\n"
"s_branch LLOOP_%=\n"
"LTAIL_%=:\n"
"ds_read_b64_tr_b16 v[96:97], v188 offset:8192\n"
"ds_read_b64_tr_b16 v[98:99], v188 offset:12288\n"
"ds_read_b64_tr_b16 v[100:101], v189 offset:8192\n"
"ds_read_b64_tr_b16 v[102:103], v189 offset:12288\n"
"ds_read_b64_tr_b16 v[104:105], v190 offset:8192\n"
"ds_read_b64_tr_b16 v[106:107], v190 offset:12288\n"
"ds_read_b64_tr_b16 v[108:109], v191 offset:8192\n"
"ds_read_b64_tr_b16 v[110:111], v191 offset:12288\n"
"v_exp_f32 v157, v157\n"
"v_exp_f32 v158, v158\n"
"v_exp_f32 v159, v159\n"
"s_waitcnt vmcnt(0)\n"
"v_lshlrev_b32 v212, 4, v196\n"
"v_add_u32 v210, s36, v212\n"
"v_mfma_f32_16x16x32_bf16 v[164:167], v[176:179], v[112:115], v[164:167]\n"
"v_cvt_pk_bf16_f32 v132, v152, v153\n"
"s_lshl_b32 s58, s57, 2\n"
"s_add_u32 s59, %[lds], 147456\n"
"v_mfma_f32_16x16x32_bf16 v[168:171], v[176:179], v[128:131], v[168:171]\n"
"v_cvt_pk_bf16_f32 v133, v154, v155\n"
"v_lshlrev_b32 v208, 4, v198\n"
"v_add_u32 v208, s59, v208\n"
"s_waitcnt lgkmcnt(10)\n"
"v_mfma_f32_16x16x32_bf16 a[0:3], v[88:91], v[112:115], a[0:3]\n"
"v_cvt_pk_bf16_f32 v134, v156, v157\n"
"v_add_u32 v209, s58, v208\n"
"s_lshl_b32 s58, s57, 12\n"
"s_waitcnt lgkmcnt(8)\n"
"v_mfma_f32_16x16x32_bf16 a[24:27], v[92:95], v[112:115], a[24:27]\n"
"v_cvt_pk_bf16_f32 v135, v158, v159\n"
"s_add_u32 s66, %[lds], 131072\n"
"s_add_u32 s58, s58, s66\n"
"v_mfma_f32_16x16x32_bf16 v[0:3], v[80:83], v[112:115], v[0:3]\n"
"v_add_u32 v211, s58, v212\n"
"v_mov_b32 v216, v160\n"
"v_mov_b32 v217, v161\n"
"v_mfma_f32_16x16x32_bf16 v[4:7], v[84:87], v[112:115], v[4:7]\n"
"v_mov_b32 v218, v162\n"
"v_permlane32_swap_b32 v160, v216\n"
"v_permlane32_swap_b32 v161, v217\n"
"v_mfma_f32_16x16x32_bf16 v[172:175], v[176:179], v[132:135], v[172:175]\n"
"ds_write_b128 v210, a[0:3] offset:0\n"
"s_waitcnt lgkmcnt(7)\n"
"v_mfma_f32_16x16x32_bf16 a[48:51], v[96:99], v[112:115], a[48:51]\n"
"v_permlane32_swap_b32 v162, v218\n"
"v_add_f32 v160, v160, v216\n"
"v_add_f32 v161, v161, v217\n"
"s_waitcnt lgkmcnt(5)\n"
"v_mfma_f32_16x16x32_bf16 a[72:75], v[100:103], v[112:115], a[72:75]\n"
"ds_write_b128 v210, a[24:27] offset:1024\n"
"s_waitcnt lgkmcnt(4)\n"
"v_mfma_f32_16x16x32_bf16 a[96:99], v[104:107], v[112:115], a[96:99]\n"
"v_add_f32 v162, v162, v218\n"
"v_mov_b32 v216, v160\n"
"v_mov_b32 v217, v161\n"
"s_waitcnt lgkmcnt(2)\n"
"v_mfma_f32_16x16x32_bf16 a[120:123], v[108:111], v[112:115], a[120:123]\n"
"ds_write_b128 v210, a[48:51] offset:12288\n"
"v_mfma_f32_16x16x32_bf16 a[4:7], v[88:91], v[116:119], a[4:7]\n"
"v_mov_b32 v218, v162\n"
"v_permlane16_swap_b32 v160, v216\n"
"v_permlane16_swap_b32 v161, v217\n"
"v_mfma_f32_16x16x32_bf16 a[28:31], v[92:95], v[116:119], a[28:31]\n"
"ds_write_b128 v210, a[72:75] offset:13312\n"
"v_mfma_f32_16x16x32_bf16 a[52:55], v[96:99], v[116:119], a[52:55]\n"
"v_permlane16_swap_b32 v162, v218\n"
"v_add_f32 v160, v160, v216\n"
"v_add_f32 v161, v161, v217\n"
"v_mfma_f32_16x16x32_bf16 a[76:79], v[100:103], v[116:119], a[76:79]\n"
"ds_write_b128 v210, a[96:99] offset:24576\n"
"v_mfma_f32_16x16x32_bf16 a[100:103], v[104:107], v[116:119], a[100:103]\n"
"v_add_f32 v162, v162, v218\n"
"s_mov_b32 s40, 0x2b800000\n"
"s_mov_b32 s41, 0x53800000\n"
"v_mfma_f32_16x16x32_bf16 a[124:127], v[108:111], v[116:119], a[124:127]\n"
"ds_write_b32 v209, v164 offset:0\n"
"ds_write_b32 v209, v160 offset:256\n"
"ds_write_b32 v209, v161 offset:512\n"
"ds_write_b32 v209, v162 offset:768\n"
"ds_write_b32 v209, v168 offset:1024\n"
"ds_write_b32 v209, v172 offset:1280\n"
"v_mfma_f32_16x16x32_bf16 v[8:11], v[80:83], v[116:119], v[8:11]\n"
"ds_write_b128 v210, a[120:123] offset:25600\n"
"v_mfma_f32_16x16x32_bf16 v[12:15], v[84:87], v[116:119], v[12:15]\n"
"ds_write_b128 v210, a[4:7] offset:2048\n"
"v_mfma_f32_16x16x32_bf16 a[8:11], v[88:91], v[120:123], a[8:11]\n"
"ds_write_b128 v210, a[28:31] offset:3072\n"
"v_mfma_f32_16x16x32_bf16 a[32:35], v[92:95], v[120:123], a[32:35]\n"
"s_waitcnt lgkmcnt(3)\n"
"s_barrier\n"
"ds_read_b128 v[216:219], v208 offset:0\n"
"ds_read_b128 v[220:223], v208 offset:256\n"
"ds_read_b128 v[224:227], v208 offset:512\n"
"ds_read_b128 v[228:231], v208 offset:768\n"
"ds_read_b128 v[232:235], v208 offset:1024\n"
"ds_read_b128 v[236:239], v208 offset:1280\n"
"v_mfma_f32_16x16x32_bf16 a[56:59], v[96:99], v[120:123], a[56:59]\n"
"s_waitcnt lgkmcnt(0)\n"
"v_add_f32 v216, v216, v217\n"
"v_add_f32 v218, v218, v219\n"
"v_add_f32 v220, v220, v221\n"
"v_mfma_f32_16x16x32_bf16 a[80:83], v[100:103], v[120:123], a[80:83]\n"
"ds_write_b128 v210, a[52:55] offset:14336\n"
"v_mfma_f32_16x16x32_bf16 a[104:107], v[104:107], v[120:123], a[104:107]\n"
"v_add_f32 v222, v222, v223\n"
"v_add_f32 v224, v224, v225\n"
"v_add_f32 v226, v226, v227\n"
"v_mfma_f32_16x16x32_bf16 a[128:131], v[108:111], v[120:123], a[128:131]\n"
"ds_write_b128 v210, a[76:79] offset:15360\n"
"v_mfma_f32_16x16x32_bf16 v[16:19], v[80:83], v[120:123], v[16:19]\n"
"v_add_f32 v228, v228, v229\n"
"v_add_f32 v230, v230, v231\n"
"v_add_f32 v232, v232, v233\n"
"v_mfma_f32_16x16x32_bf16 v[20:23], v[84:87], v[120:123], v[20:23]\n"
"ds_write_b128 v210, a[100:103] offset:26624\n"
"v_mfma_f32_16x16x32_bf16 a[12:15], v[88:91], v[124:127], a[12:15]\n"
"v_add_f32 v234, v234, v235\n"
"v_add_f32 v236, v236, v237\n"
"v_add_f32 v238, v238, v239\n"
"v_mfma_f32_16x16x32_bf16 a[36:39], v[92:95], v[124:127], a[36:39]\n"
"ds_write_b128 v210, a[124:127] offset:27648\n"
"v_mfma_f32_16x16x32_bf16 a[60:63], v[96:99], v[124:127], a[60:63]\n"
"v_add_f32 v216, v216, v218\n"
"v_add_f32 v220, v220, v222\n"
"v_add_f32 v224, v224, v226\n"
"v_mfma_f32_16x16x32_bf16 a[84:87], v[100:103], v[124:127], a[84:87]\n"
"ds_write_b128 v210, a[8:11] offset:4096\n"
"v_mfma_f32_16x16x32_bf16 a[108:111], v[104:107], v[124:127], a[108:111]\n"
"v_add_f32 v228, v228, v230\n"
"v_add_f32 v232, v232, v234\n"
"v_add_f32 v236, v236, v238\n"
"v_mfma_f32_16x16x32_bf16 a[132:135], v[108:111], v[124:127], a[132:135]\n"
"ds_write_b128 v210, a[32:35] offset:5120\n"
"v_mfma_f32_16x16x32_bf16 v[24:27], v[80:83], v[124:127], v[24:27]\n"
"v_min3_f32 v221, v216, v220, v224\n"
"v_add_f32 v217, v216, v220\n"
"v_min3_f32 v221, v221, v228, v232\n"
"v_mfma_f32_16x16x32_bf16 v[28:31], v[84:87], v[124:127], v[28:31]\n"
"ds_write_b128 v210, a[56:59] offset:16384\n"
"v_mfma_f32_16x16x32_bf16 a[16:19], v[88:91], v[128:131], a[16:19]\n"
"v_add_f32 v218, v224, v228\n"
"v_min_f32 v221, v221, v236\n"
"v_add_f32 v219, v232, v236\n"
"v_mfma_f32_16x16x32_bf16 a[40:43], v[92:95], v[128:131], a[40:43]\n"
"ds_write_b128 v210, a[80:83] offset:17408\n"
"v_mfma_f32_16x16x32_bf16 a[64:67], v[96:99], v[128:131], a[64:67]\n"
"v_add_f32 v217, v217, v218\n"
"v_cmp_nle_f32 s[42:43], s40, v221\n"
"v_add_f32 v217, v217, v219\n"
"v_mfma_f32_16x16x32_bf16 a[88:91], v[100:103], v[128:131], a[88:91]\n"
"ds_write_b128 v210, a[104:107] offset:28672\n"
"v_mfma_f32_16x16x32_bf16 a[112:115], v[104:107], v[128:131], a[112:115]\n"
"v_rcp_f32 v200, v216\n"
"v_rcp_f32 v201, v220\n"
"v_rcp_f32 v202, v224\n"
"v_mfma_f32_16x16x32_bf16 a[136:139], v[108:111], v[128:131], a[136:139]\n"
"ds_write_b128 v210, a[128:131] offset:29696\n"
"v_mfma_f32_16x16x32_bf16 v[32:35], v[80:83], v[128:131], v[32:35]\n"
"v_cmp_nge_f32 vcc, s41, v217\n"
"v_rcp_f32 v203, v228\n"
"v_rcp_f32 v204, v232\n"
"v_mfma_f32_16x16x32_bf16 v[36:39], v[84:87], v[128:131], v[36:39]\n"
"ds_write_b128 v210, a[12:15] offset:6144\n"
"v_mfma_f32_16x16x32_bf16 a[20:23], v[88:91], v[132:135], a[20:23]\n"
"v_rcp_f32 v205, v236\n"
"s_or_b64 s[38:39], s[42:43], vcc\n"
"s_cmp_lg_u64 s[38:39], 0\n"
"s_cselect_b32 %[flag], 1, 0\n"
"s_cbranch_scc1 LEND_%=\n"
"v_mfma_f32_16x16x32_bf16 a[44:47], v[92:95], v[132:135], a[44:47]\n"
"ds_write_b128 v210, a[36:39] offset:7168\n"
"v_mfma_f32_16x16x32_bf16 a[68:71], v[96:99], v[132:135], a[68:71]\n"
"s_sub_u32 s58, s57, 1\n"
"s_and_b32 s58, s58, 3\n"
"s_lshl_b32 s59, s58, 15\n"
"v_mfma_f32_16x16x32_bf16 a[92:95], v[100:103], v[132:135], a[92:95]\n"
"ds_write_b128 v210, a[60:63] offset:18432\n"
"v_mfma_f32_16x16x32_bf16 a[116:119], v[104:107], v[132:135], a[116:119]\n"
"s_add_u32 s59, s59, %[lds]\n"
"v_add_u32 v213, s59, v212\n"
"s_sub_u32 s58, s57, 2\n"
"v_mfma_f32_16x16x32_bf16 a[140:143], v[108:111], v[132:135], a[140:143]\n"
"ds_write_b128 v210, a[84:87] offset:19456\n"
"v_mfma_f32_16x16x32_bf16 v[40:43], v[80:83], v[132:135], v[40:43]\n"
"s_and_b32 s58, s58, 3\n"
"s_lshl_b32 s59, s58, 15\n"
"s_add_u32 s59, s59, %[lds]\n"
"v_mfma_f32_16x16x32_bf16 v[44:47], v[84:87], v[132:135], v[44:47]\n"
"ds_write_b128 v210, a[108:111] offset:30720\n"
"v_add_u32 v214, s59, v212\n"
"s_sub_u32 s58, s57, 3\n"
"s_and_b32 s58, s58, 3\n"
"s_lshl_b32 s59, s58, 15\n"
"s_add_u32 s59, s59, %[lds]\n"
"s_nop 2\n"
"ds_write_b128 v210, a[132:135] offset:31744\n"
"v_add_u32 v215, s59, v212\n"
"s_lshl_b32 s59, s58, 12\n"
"s_add_u32 s59, s59, s66\n"
"v_add_u32 v240, s59, v212\n"
"v_and_b32 v206, 1, v199\n"
"ds_write_b128 v210, a[16:19] offset:8192\n"
"v_lshlrev_b32 v206, 5, v206\n"
"v_lshrrev_b32 v207, 1, v199\n"
"v_lshl_add_u32 v206, v207, 4, v206\n"
"v_lshl_add_u32 v243, v198, 8, v206\n"
"s_lshl_b32 s58, s57, 6\n"
"ds_write_b128 v210, a[40:43] offset:9216\n"
"v_add_u32 v243, s58, v243\n"
"s_mov_b32 s28, s54\n"
"s_mov_b32 s29, s55\n"
"s_mov_b32 s61, 4096\n"
"s_mov_b32 s62, 8192\n"
"ds_write_b128 v210, a[64:67] offset:20480\n"
"s_mov_b32 s63, 12288\n"
"s_mov_b32 s64, 16384\n"
"s_mov_b32 s65, 20480\n"
"ds_write_b128 v210, a[88:91] offset:21504\n"
"ds_write_b128 v211, a[112:115] offset:0\n"
"ds_write_b128 v211, a[136:139] offset:1024\n"
"ds_write_b128 v210, a[20:23] offset:10240\n"
"ds_write_b128 v210, a[44:47] offset:11264\n"
"ds_write_b128 v210, a[68:71] offset:22528\n"
"ds_write_b128 v210, a[92:95] offset:23552\n"
"ds_write_b128 v211, a[116:119] offset:2048\n"
"ds_write_b128 v211, a[140:143] offset:3072\n"
"s_waitcnt lgkmcnt(0)\n"
"s_barrier\n"
"ds_read_b128 v[48:51], v213 offset:0\n"
"ds_read_b128 v[52:55], v213 offset:1024\n"
"ds_read_b128 v[56:59], v214 offset:12288\n"
"ds_read_b128 v[60:63], v214 offset:13312\n"
"ds_read_b128 v[64:67], v215 offset:24576\n"
"ds_read_b128 v[68:71], v215 offset:25600\n"
"ds_read_b128 v[72:75], v213 offset:2048\n"
"ds_read_b128 v[76:79], v213 offset:3072\n"
"ds_read_b128 v[80:83], v214 offset:14336\n"
"ds_read_b128 v[84:87], v214 offset:15360\n"
"ds_read_b128 v[88:91], v215 offset:26624\n"
"ds_read_b128 v[92:95], v215 offset:27648\n"
"ds_read_b128 v[96:99], v213 offset:4096\n"
"ds_read_b128 v[100:103], v213 offset:5120\n"
"ds_read_b128 v[104:107], v214 offset:16384\n"
"ds_read_b128 v[108:111], v214 offset:17408\n"
"ds_read_b128 v[112:115], v215 offset:28672\n"
"ds_read_b128 v[116:119], v215 offset:29696\n"
"s_waitcnt lgkmcnt(12)\n"
"v_pk_add_f32 v[0:1], v[0:1], v[48:49]\n"
"v_pk_add_f32 v[2:3], v[2:3], v[50:51]\n"
"v_pk_add_f32 v[4:5], v[4:5], v[52:53]\n"
"v_pk_add_f32 v[6:7], v[6:7], v[54:55]\n"
"v_pk_add_f32 v[56:57], v[56:57], v[64:65]\n"
"v_pk_add_f32 v[58:59], v[58:59], v[66:67]\n"
"v_pk_add_f32 v[60:61], v[60:61], v[68:69]\n"
"v_pk_add_f32 v[62:63], v[62:63], v[70:71]\n"
"v_pk_add_f32 v[0:1], v[0:1], v[56:57]\n"
"v_pk_add_f32 v[2:3], v[2:3], v[58:59]\n"
"v_pk_add_f32 v[4:5], v[4:5], v[60:61]\n"
"v_pk_add_f32 v[6:7], v[6:7], v[62:63]\n"
"ds_read_b128 v[120:123], v213 offset:6144\n"
"ds_read_b128 v[124:127], v213 offset:7168\n"
"ds_read_b128 v[128:131], v214 offset:18432\n"
"ds_read_b128 v[132:135], v214 offset:19456\n"
"ds_read_b128 v[136:139], v215 offset:30720\n"
"ds_read_b128 v[140:143], v215 offset:31744\n"
"v_pk_mul_f32 v[0:1], v[0:1], v[200:201] op_sel:[0,0] op_sel_hi:[1,0]\n"
"v_pk_mul_f32 v[2:3], v[2:3], v[200:201] op_sel:[0,0] op_sel_hi:[1,0]\n"
"v_pk_mul_f32 v[4:5], v[4:5], v[200:201] op_sel:[0,0] op_sel_hi:[1,0]\n"
"v_pk_mul_f32 v[6:7], v[6:7], v[200:201] op_sel:[0,0] op_sel_hi:[1,0]\n"
"v_cvt_pk_bf16_f32 v144, v0, v1\n"
"v_cvt_pk_bf16_f32 v145, v2, v3\n"
"v_cvt_pk_bf16_f32 v146, v4, v5\n"
"v_cvt_pk_bf16_f32 v147, v6, v7\n"
"s_waitcnt lgkmcnt(12)\n"
"v_pk_add_f32 v[8:9], v[8:9], v[72:73]\n"
"v_pk_add_f32 v[10:11], v[10:11], v[74:75]\n"
"v_pk_add_f32 v[12:13], v[12:13], v[76:77]\n"
"v_pk_add_f32 v[14:15], v[14:15], v[78:79]\n"
"v_permlane16_swap_b32 v144, v146\n"
"v_permlane16_swap_b32 v145, v147\n"
"v_pk_add_f32 v[80:81], v[80:81], v[88:89]\n"
"v_pk_add_f32 v[82:83], v[82:83], v[90:91]\n"
"buffer_store_dwordx4 v[144:147], v243, s[28:31], 0 offen offset:0 sc1\n"
"v_pk_add_f32 v[84:85], v[84:85], v[92:93]\n"
"v_pk_add_f32 v[86:87], v[86:87], v[94:95]\n"
"v_pk_add_f32 v[8:9], v[8:9], v[80:81]\n"
"v_pk_add_f32 v[10:11], v[10:11], v[82:83]\n"
"v_pk_add_f32 v[12:13], v[12:13], v[84:85]\n"
"v_pk_add_f32 v[14:15], v[14:15], v[86:87]\n"
"ds_read_b128 v[48:51], v213 offset:8192\n"
"ds_read_b128 v[52:55], v213 offset:9216\n"
"ds_read_b128 v[56:59], v214 offset:20480\n"
"ds_read_b128 v[60:63], v214 offset:21504\n"
"ds_read_b128 v[64:67], v240 offset:0\n"
"ds_read_b128 v[68:71], v240 offset:1024\n"
"v_pk_mul_f32 v[8:9], v[8:9], v[200:201] op_sel:[0,1] op_sel_hi:[1,1]\n"
"v_pk_mul_f32 v[10:11], v[10:11], v[200:201] op_sel:[0,1] op_sel_hi:[1,1]\n"
"v_pk_mul_f32 v[12:13], v[12:13], v[200:201] op_sel:[0,1] op_sel_hi:[1,1]\n"
"v_pk_mul_f32 v[14:15], v[14:15], v[200:201] op_sel:[0,1] op_sel_hi:[1,1]\n"
"v_cvt_pk_bf16_f32 v148, v8, v9\n"
"v_cvt_pk_bf16_f32 v149, v10, v11\n"
"v_cvt_pk_bf16_f32 v150, v12, v13\n"
"v_cvt_pk_bf16_f32 v151, v14, v15\n"
"s_waitcnt lgkmcnt(12)\n"
"v_pk_add_f32 v[16:17], v[16:17], v[96:97]\n"
"v_pk_add_f32 v[18:19], v[18:19], v[98:99]\n"
"v_pk_add_f32 v[20:21], v[20:21], v[100:101]\n"
"v_pk_add_f32 v[22:23], v[22:23], v[102:103]\n"
"v_permlane16_swap_b32 v148, v150\n"
"v_permlane16_swap_b32 v149, v151\n"
"v_pk_add_f32 v[104:105], v[104:105], v[112:113]\n"
"v_pk_add_f32 v[106:107], v[106:107], v[114:115]\n"
"buffer_store_dwordx4 v[148:151], v243, s[28:31], s61 offen offset:0 sc1\n"
"v_pk_add_f32 v[108:109], v[108:109], v[116:117]\n"
"v_pk_add_f32 v[110:111], v[110:111], v[118:119]\n"
"v_pk_add_f32 v[16:17], v[16:17], v[104:105]\n"
"v_pk_add_f32 v[18:19], v[18:19], v[106:107]\n"
"v_pk_add_f32 v[20:21], v[20:21], v[108:109]\n"
"v_pk_add_f32 v[22:23], v[22:23], v[110:111]\n"
"ds_read_b128 v[72:75], v213 offset:10240\n"
"ds_read_b128 v[76:79], v213 offset:11264\n"
"ds_read_b128 v[80:83], v214 offset:22528\n"
"ds_read_b128 v[84:87], v214 offset:23552\n"
"ds_read_b128 v[88:91], v240 offset:2048\n"
"ds_read_b128 v[92:95], v240 offset:3072\n"
"v_pk_mul_f32 v[16:17], v[16:17], v[202:203] op_sel:[0,0] op_sel_hi:[1,0]\n"
"v_pk_mul_f32 v[18:19], v[18:19], v[202:203] op_sel:[0,0] op_sel_hi:[1,0]\n"
"v_pk_mul_f32 v[20:21], v[20:21], v[202:203] op_sel:[0,0] op_sel_hi:[1,0]\n"
"v_pk_mul_f32 v[22:23], v[22:23], v[202:203] op_sel:[0,0] op_sel_hi:[1,0]\n"
"v_cvt_pk_bf16_f32 v152, v16, v17\n"
"v_cvt_pk_bf16_f32 v153, v18, v19\n"
"v_cvt_pk_bf16_f32 v154, v20, v21\n"
"v_cvt_pk_bf16_f32 v155, v22, v23\n"
"s_waitcnt lgkmcnt(12)\n"
"v_pk_add_f32 v[24:25], v[24:25], v[120:121]\n"
"v_pk_add_f32 v[26:27], v[26:27], v[122:123]\n"
"v_pk_add_f32 v[28:29], v[28:29], v[124:125]\n"
"v_pk_add_f32 v[30:31], v[30:31], v[126:127]\n"
"v_permlane16_swap_b32 v152, v154\n"
"v_permlane16_swap_b32 v153, v155\n"
"v_pk_add_f32 v[128:129], v[128:129], v[136:137]\n"
"v_pk_add_f32 v[130:131], v[130:131], v[138:139]\n"
"buffer_store_dwordx4 v[152:155], v243, s[28:31], s62 offen offset:0 sc1\n"
"v_pk_add_f32 v[132:133], v[132:133], v[140:141]\n"
"v_pk_add_f32 v[134:135], v[134:135], v[142:143]\n"
"v_pk_add_f32 v[24:25], v[24:25], v[128:129]\n"
"v_pk_add_f32 v[26:27], v[26:27], v[130:131]\n"
"v_pk_add_f32 v[28:29], v[28:29], v[132:133]\n"
"v_pk_add_f32 v[30:31], v[30:31], v[134:135]\n"
"v_pk_mul_f32 v[24:25], v[24:25], v[202:203] op_sel:[0,1] op_sel_hi:[1,1]\n"
"v_pk_mul_f32 v[26:27], v[26:27], v[202:203] op_sel:[0,1] op_sel_hi:[1,1]\n"
"v_pk_mul_f32 v[28:29], v[28:29], v[202:203] op_sel:[0,1] op_sel_hi:[1,1]\n"
"v_pk_mul_f32 v[30:31], v[30:31], v[202:203] op_sel:[0,1] op_sel_hi:[1,1]\n"
"v_cvt_pk_bf16_f32 v144, v24, v25\n"
"v_cvt_pk_bf16_f32 v145, v26, v27\n"
"v_cvt_pk_bf16_f32 v146, v28, v29\n"
"v_cvt_pk_bf16_f32 v147, v30, v31\n"
"s_waitcnt lgkmcnt(6)\n"
"v_pk_add_f32 v[32:33], v[32:33], v[48:49]\n"
"v_pk_add_f32 v[34:35], v[34:35], v[50:51]\n"
"v_pk_add_f32 v[36:37], v[36:37], v[52:53]\n"
"v_pk_add_f32 v[38:39], v[38:39], v[54:55]\n"
"v_permlane16_swap_b32 v144, v146\n"
"v_permlane16_swap_b32 v145, v147\n"
"v_pk_add_f32 v[56:57], v[56:57], v[64:65]\n"
"v_pk_add_f32 v[58:59], v[58:59], v[66:67]\n"
"buffer_store_dwordx4 v[144:147], v243, s[28:31], s63 offen offset:0 sc1\n"
"v_pk_add_f32 v[60:61], v[60:61], v[68:69]\n"
"v_pk_add_f32 v[62:63], v[62:63], v[70:71]\n"
"v_pk_add_f32 v[32:33], v[32:33], v[56:57]\n"
"v_pk_add_f32 v[34:35], v[34:35], v[58:59]\n"
"v_pk_add_f32 v[36:37], v[36:37], v[60:61]\n"
"v_pk_add_f32 v[38:39], v[38:39], v[62:63]\n"
"v_pk_mul_f32 v[32:33], v[32:33], v[204:205] op_sel:[0,0] op_sel_hi:[1,0]\n"
"v_pk_mul_f32 v[34:35], v[34:35], v[204:205] op_sel:[0,0] op_sel_hi:[1,0]\n"
"v_pk_mul_f32 v[36:37], v[36:37], v[204:205] op_sel:[0,0] op_sel_hi:[1,0]\n"
"v_pk_mul_f32 v[38:39], v[38:39], v[204:205] op_sel:[0,0] op_sel_hi:[1,0]\n"
"v_cvt_pk_bf16_f32 v148, v32, v33\n"
"v_cvt_pk_bf16_f32 v149, v34, v35\n"
"v_cvt_pk_bf16_f32 v150, v36, v37\n"
"v_cvt_pk_bf16_f32 v151, v38, v39\n"
"s_waitcnt lgkmcnt(0)\n"
"v_pk_add_f32 v[40:41], v[40:41], v[72:73]\n"
"v_pk_add_f32 v[42:43], v[42:43], v[74:75]\n"
"v_pk_add_f32 v[44:45], v[44:45], v[76:77]\n"
"v_pk_add_f32 v[46:47], v[46:47], v[78:79]\n"
"v_permlane16_swap_b32 v148, v150\n"
"v_permlane16_swap_b32 v149, v151\n"
"v_pk_add_f32 v[80:81], v[80:81], v[88:89]\n"
"v_pk_add_f32 v[82:83], v[82:83], v[90:91]\n"
"buffer_store_dwordx4 v[148:151], v243, s[28:31], s64 offen offset:0 sc1\n"
"v_pk_add_f32 v[84:85], v[84:85], v[92:93]\n"
"v_pk_add_f32 v[86:87], v[86:87], v[94:95]\n"
"v_pk_add_f32 v[40:41], v[40:41], v[80:81]\n"
"v_pk_add_f32 v[42:43], v[42:43], v[82:83]\n"
"v_pk_add_f32 v[44:45], v[44:45], v[84:85]\n"
"v_pk_add_f32 v[46:47], v[46:47], v[86:87]\n"
"v_pk_mul_f32 v[40:41], v[40:41], v[204:205] op_sel:[0,1] op_sel_hi:[1,1]\n"
"v_pk_mul_f32 v[42:43], v[42:43], v[204:205] op_sel:[0,1] op_sel_hi:[1,1]\n"
"v_pk_mul_f32 v[44:45], v[44:45], v[204:205] op_sel:[0,1] op_sel_hi:[1,1]\n"
"v_pk_mul_f32 v[46:47], v[46:47], v[204:205] op_sel:[0,1] op_sel_hi:[1,1]\n"
"v_cvt_pk_bf16_f32 v152, v40, v41\n"
"v_cvt_pk_bf16_f32 v153, v42, v43\n"
"v_cvt_pk_bf16_f32 v154, v44, v45\n"
"v_cvt_pk_bf16_f32 v155, v46, v47\n"
"s_nop 0\n"
"v_permlane16_swap_b32 v152, v154\n"
"v_permlane16_swap_b32 v153, v155\n"
"s_nop 0\n"
"buffer_store_dwordx4 v[152:155], v243, s[28:31], s65 offen offset:0 sc1\n"
"LEND_%=:\n"
      : [flag] "=&s"(flag)
      : [ka] "s"(ka), [wg] "s"(wg), [tid] "v"(threadIdx.x), [lds] "s"(lds)
      : "memory", "scc", "vcc", "s8","s9","s10","s11","s12","s13","s14","s15","s16","s17","s18","s19","s20","s21","s22","s23","s24","s25","s26","s27","s28","s29","s30","s31","s35","s36","s37","s38","s39","s40","s41","s42","s43","s44","s45","s46","s47","s48","s49","s50","s51","s52","s53","s54","s55","s56","s57","s58","s59","s60","s61","s62","s63","s64","s65","s66","s67","s68","s69","s70","s71","s72","s73","s74","s75","s76","s77","s78","s79","s80","s81","s82","s83","s84","s85","s86","s87","s88","s89","s90","s91","s92","s93","s94","s95","s96","s97","s98","s99", "v0","v1","v2","v3","v4","v5","v6","v7","v8","v9","v10","v11","v12","v13","v14","v15","v16","v17","v18","v19","v20","v21","v22","v23","v24","v25","v26","v27","v28","v29","v30","v31","v32","v33","v34","v35","v36","v37","v38","v39","v40","v41","v42","v43","v44","v45","v46","v47","v48","v49","v50","v51","v52","v53","v54","v55","v56","v57","v58","v59","v60","v61","v62","v63","v64","v65","v66","v67","v68","v69","v70","v71","v72","v73","v74","v75","v76","v77","v78","v79","v80","v81","v82","v83","v84","v85","v86","v87","v88","v89","v90","v91","v92","v93","v94","v95","v96","v97","v98","v99","v100","v101","v102","v103","v104","v105","v106","v107","v108","v109","v110","v111","v112","v113","v114","v115","v116","v117","v118","v119","v120","v121","v122","v123","v124","v125","v126","v127","v128","v129","v130","v131","v132","v133","v134","v135","v136","v137","v138","v139","v140","v141","v142","v143","v144","v145","v146","v147","v148","v149","v150","v151","v152","v153","v154","v155","v156","v157","v158","v159","v160","v161","v162","v163","v164","v165","v166","v167","v168","v169","v170","v171","v172","v173","v174","v175","v176","v177","v178","v179","v180","v181","v182","v183","v184","v185","v186","v187","v188","v189","v190","v191","v192","v193","v194","v195","v196","v197","v198","v199","v200","v201","v202","v203","v204","v205","v206","v207","v208","v209","v210","v211","v212","v213","v214","v215","v216","v217","v218","v219","v220","v221","v222","v223","v224","v225","v226","v227","v228","v229","v230","v231","v232","v233","v234","v235","v236","v237","v238","v239","v240","v241","v242","v243","v244","v245","v246","v247", "a0","a1","a2","a3","a4","a5","a6","a7","a8","a9","a10","a11","a12","a13","a14","a15","a16","a17","a18","a19","a20","a21","a22","a23","a24","a25","a26","a27","a28","a29","a30","a31","a32","a33","a34","a35","a36","a37","a38","a39","a40","a41","a42","a43","a44","a45","a46","a47","a48","a49","a50","a51","a52","a53","a54","a55","a56","a57","a58","a59","a60","a61","a62","a63","a64","a65","a66","a67","a68","a69","a70","a71","a72","a73","a74","a75","a76","a77","a78","a79","a80","a81","a82","a83","a84","a85","a86","a87","a88","a89","a90","a91","a92","a93","a94","a95","a96","a97","a98","a99","a100","a101","a102","a103","a104","a105","a106","a107","a108","a109","a110","a111","a112","a113","a114","a115","a116","a117","a118","a119","a120","a121","a122","a123","a124","a125","a126","a127","a128","a129","a130","a131","a132","a133","a134","a135","a136","a137","a138","a139","a140","a141","a142","a143","a144","a145","a146","a147","a148","a149","a150","a151","a152","a153","a154","a155","a156","a157","a158","a159","a160","a161","a162","a163","a164","a165","a166","a167","a168","a169","a170","a171","a172","a173","a174","a175","a176","a177","a178","a179","a180","a181","a182","a183","a184","a185","a186","a187","a188","a189","a190","a191","a192","a193","a194","a195","a196","a197","a198","a199","a200","a201","a202","a203","a204","a205","a206","a207","a208","a209","a210","a211","a212","a213","a214","a215","a216","a217","a218","a219","a220","a221","a222","a223","a224","a225","a226","a227","a228","a229","a230","a231","a232","a233","a234","a235","a236","a237","a238","a239","a240","a241","a242","a243","a244","a245","a246","a247","a248","a249","a250","a251","a252","a253","a254","a255");
  if (flag) {
    unsigned long long p0, p1, p2, p3;
    asm volatile("s_load_dwordx2 %0, %4, 0x0\n\ts_load_dwordx2 %1, %4, 0x8\n\ts_load_dwordx2 %2, %4, 0x10\n\ts_load_dwordx2 %3, %4, 0x18\n\ts_waitcnt lgkmcnt(0)"
                 : "=&s"(p0), "=&s"(p1), "=&s"(p2), "=&s"(p3) : "s"(ka) : "memory");
    unsigned lane;
    asm volatile("v_mbcnt_lo_u32_b32 %0, -1, 0\n\tv_mbcnt_hi_u32_b32 %0, -1, %0" : "=v"(lane));
    slow_path((const u16*)p0, (const u16*)p1, (const u16*)p2, (u16*)p3, wg, wave, lane);
  }
}

extern "C" void kernel_launch(void* const* d_in, const int* in_sizes, int n_in, void* d_out, int out_size, void* d_ws, size_t ws_size,
                              hipStream_t stream) {
  gqa_attn_fwd<<<256, 256, 0, stream>>>((const u16*)d_in[0], (const u16*)d_in[1], (const u16*)d_in[2], (u16*)d_out);
}
```
